```python
import torch

def _softmax(x, axis=-1):
    return torch.softmax(x, dim=axis)
def _gelu(x):
    return torch.nn.functional.gelu(x, approximate="tanh")
def _silu(x):
    return torch.nn.functional.silu(x)
def _sigmoid(x):
    return torch.sigmoid(x)
def _layernorm(x, axis=-1, eps=1e-5):
    m = x.mean(dim=axis, keepdim=True)
    v = x.var(dim=axis, keepdim=True, unbiased=False)
    return (x - m) / torch.sqrt(v + eps)
def _scatter_add(tgt, idx, src, axis=0):
    return tgt.clone().index_add_(axis, idx, src)
def _segment_sum(src, seg, n):
    out = torch.zeros((n,) + src.shape[1:], dtype=src.dtype)
    return out.index_add_(0, seg, src)
def _pad(x, pw):
    flat = tuple(v for pair in reversed(pw) for v in pair)
    return torch.nn.functional.pad(x, flat)

kernel_name = 'concat_heads__linear_attn_block__grouped_kv_mqa__prompt'

def setup_inputs():

    _w = (lambda a: (lambda t: t.to(torch.bfloat16) if t.is_floating_point() else t)(torch.as_tensor(a)))
    def _A_setup():
        B, S, D = 1, 2048, 512
        return {
            "x": _w((lambda *s: torch.randn(*s, dtype=torch.bfloat16) * 0.05)(B, S, D)),
            "wqkv": _w((lambda *s: torch.randn(*s, dtype=torch.bfloat16) * 0.8)(D, 3 * D)),
            "wo": _w((lambda *s: torch.randn(*s, dtype=torch.bfloat16) * 0.05)(D, D)),
        }
    _A_in = _A_setup()

    def _B_setup():
        B, S, D, H = 1, 2048, 512, 32
        Dh = D // H
        return {
            "x": _w((lambda *s: torch.randn(*s, dtype=torch.bfloat16) * 0.05)(B, S, D)),
            "wq": _w((lambda *s: torch.randn(*s, dtype=torch.bfloat16) * 1.6)(D, H * Dh)),
            "wk": _w((lambda *s: torch.randn(*s, dtype=torch.bfloat16) * 1.6)(D, Dh)),
            "wv": _w((lambda *s: torch.randn(*s, dtype=torch.bfloat16) * 0.05)(D, Dh)),
            "wo": _w((lambda *s: torch.randn(*s, dtype=torch.bfloat16) * 0.05)(H * Dh, D)),
        }
    _B_in = _B_setup()

    return {
        "A_x": _A_in["x"],
        "A_wqkv": _A_in["wqkv"],
        "A_wo": _A_in["wo"],
        "B_x": _B_in["x"],
        "B_wq": _B_in["wq"],
        "B_wk": _B_in["wk"],
        "B_wv": _B_in["wv"],
        "B_wo": _B_in["wo"],
    }

def reference(A_x, A_wqkv, A_wo, B_x, B_wq, B_wk, B_wv, B_wo):
    def _A_ref(x, wqkv, wo):
        B, S, D = x.shape
        H = 32; Dh = D // H
        qkv = (lambda x, *d: x.permute(*d))((x @ wqkv).reshape(B, S, 3, H, Dh), 2, 0, 3, 1, 4)
        q, k, v = qkv[0], qkv[1], qkv[2]
        phi = lambda t: torch.where(t > 0, t + 1, torch.exp(t)) + 1e-6
        pq, pk = phi(q), phi(k)
        kv = torch.einsum("bhsd,bhse->bhde", pk, v)
        z = torch.einsum("bhsd->bhd", pk)
        num = torch.einsum("bhqd,bhde->bhqe", pq, kv)
        den = torch.einsum("bhqd,bhd->bhq", pq, z)[..., None] + 1e-6
        o = num / den
        return (lambda x, *d: x.permute(*d))(o, 0, 2, 1, 3).reshape(B, S, D) @ wo

    def _B_ref(x, wq, wk, wv, wo):
        B, S, D = x.shape
        H = 32
        Dh = D // H
        q = (lambda x, *d: x.permute(*d))((x @ wq).reshape(B, S, H, Dh), 0, 2, 1, 3)
        k = x @ wk
        v = x @ wv
        scores = torch.einsum("bhqd,bkd->bhqk", q, k) * (Dh ** -0.5)
        p = _softmax(scores, axis=-1)
        o = torch.einsum("bhqk,bkd->bhqd", p, v)
        return (lambda x, *d: x.permute(*d))(o, 0, 2, 1, 3).reshape(B, S, H * Dh) @ wo

    a = _A_ref(A_x, A_wqkv, A_wo)
    b = _B_ref(B_x, B_wq, B_wk, B_wv, B_wo)
    return torch.concatenate([a, b], axis=-1)
```

```cpp
#include <hip/hip_runtime.h>
#include <hip/hip_bf16.h>
#include <stdint.h>

typedef float f32x16 __attribute__((ext_vector_type(16)));
typedef float f32x4 __attribute__((ext_vector_type(4)));
typedef short bf16x8 __attribute__((ext_vector_type(8)));
typedef __bf16 bfv8 __attribute__((ext_vector_type(8)));
typedef unsigned u32x4 __attribute__((ext_vector_type(4)));
typedef unsigned u32x2 __attribute__((ext_vector_type(2)));
typedef unsigned short u16;
#define LAS __attribute__((address_space(3)))

constexpr int S = 2048, D = 512, H = 32;
constexpr float QSCALE = 0.25f * 1.4426950408889634f;
constexpr int KIMG = 65536, VGS = 528, VIMG = 128 * VGS, KVIMG = KIMG + VIMG;

__device__ __forceinline__ float bf2f(u16 h) { return __uint_as_float((unsigned)h << 16); }
__device__ __forceinline__ unsigned cvtpk(float lo, float hi) { unsigned r; asm("v_cvt_pk_bf16_f32 %0, %1, %2" : "=v"(r) : "v"(lo), "v"(hi)); return r; }
__device__ __forceinline__ u16 f2bf(float f) { return (u16)(cvtpk(f, f) & 0xffff); }
__device__ __forceinline__ f32x16 mfma32(u32x4 a, u32x4 b, f32x16 c) {
  return __builtin_amdgcn_mfma_f32_32x32x16_bf16(__builtin_bit_cast(bfv8, a), __builtin_bit_cast(bfv8, b), c, 0, 0, 0); }
__device__ __forceinline__ void glds16(const void* g, unsigned lds_base) {
  unsigned sv; asm volatile("s_mov_b32 %0, m0\n\ts_mov_b32 m0, %2\n\ts_nop 0\n\tglobal_load_lds_dwordx4 %1, off\n\ts_mov_b32 m0, %0" : "=&s"(sv) : "v"(g), "s"(lds_base) : "memory"); }

__global__ void __launch_bounds__(64) gemm_naive(const u16* X, const u16* W, float* C, int N, int ldc) {
  int lane = threadIdx.x, r = lane & 31, h = lane >> 5;
  int m0 = blockIdx.y * 32, n0 = blockIdx.x * 32;
  f32x16 acc = {};
  int n = n0 + r; bool ok = n < N;
  for (int k = 0; k < D; k += 16) {
    u32x4 a = *(const u32x4*)(X + (size_t)(m0 + r) * D + k + 8 * h);
    u16 w[8];
    for (int j = 0; j < 8; j++) w[j] = ok ? W[(size_t)(k + 8 * h + j) * N + n] : (u16)0;
    u32x4 b = {w[0] | (unsigned)w[1] << 16, w[2] | (unsigned)w[3] << 16, w[4] | (unsigned)w[5] << 16, w[6] | (unsigned)w[7] << 16};
    acc = mfma32(a, b, acc);
  }
  if (ok) for (int i = 0; i < 16; i++) C[(size_t)(m0 + (i & 3) + 8 * (i >> 2) + 4 * h) * ldc + n] = acc[i];
}

__global__ void layoutB(const float* q, const float* k, const float* v, u16* Qf, unsigned char* KV) {
  int i = blockIdx.x * blockDim.x + threadIdx.x;
  if (i < S * D) {
    int j = i & 7, l = (i >> 3) & 63, hd = (i >> 9) & 31, rb = i >> 14;
    int d = 8 * (j >> 2) + 4 * (l >> 5) + (j & 3);
    Qf[i] = f2bf(q[(size_t)(rb * 32 + (l & 31)) * D + hd * 16 + d] * QSCALE);
  }
  if (i < S * 16) {
    int j = i & 7, l = (i >> 3) & 63, t = i >> 9;
    int d = 8 * (j >> 2) + 4 * (l >> 5) + (j & 3);
    ((u16*)KV)[i] = f2bf(k[(size_t)(t * 32 + (l & 31)) * 16 + d]);
    int e = (i >> 3) & 15, hA = (i >> 7) & 1, g = i >> 8;
    ((u16*)(KV + KIMG + g * VGS))[i & 255] = f2bf(v[(size_t)(g * 16 + hA * 8 + j) * 16 + e]);
    if ((i & 255) < 8) ((u16*)(KV + KIMG + g * VGS + 512))[i & 255] = 0x3f80;
  }
}

__global__ void __launch_bounds__(512, 2) attn(const u32x4* __restrict__ Qf, const unsigned char* __restrict__ KV, u32x4* __restrict__ Of) {
  extern __shared__ __attribute__((aligned(16))) unsigned char lds[];
  const int tid = threadIdx.x, lane = tid & 63, w = __builtin_amdgcn_readfirstlane(tid >> 6);
  const int bi = blockIdx.x >> 3, bj = blockIdx.x & 7;
  const int p = w & 3, u = w >> 2;
  const int rb = bi * 2 + (p & 1), hd0 = bj * 4 + (p >> 1) * 2;
  const unsigned lds0 = (unsigned)(uintptr_t)lds;
  for (int c = w; c < KVIMG / 1024; c += 8) glds16(KV + c * 1024 + lane * 16, lds0 + c * 1024);
  u32x4 qx = Qf[(rb * 32 + hd0) * 64 + lane], qy = Qf[(rb * 32 + hd0 + 1) * 64 + lane];
  asm volatile("s_waitcnt vmcnt(0)" ::: "memory");
  __syncthreads();
  float mx, my;
  {
    u32x4 k0 = *(const LAS u32x4*)(lds0 + lane * 16);
    f32x16 sx = mfma32(k0, qx, f32x16{}), sy = mfma32(k0, qy, f32x16{});
    mx = sx[0]; my = sy[0];
    for (int i = 1; i < 16; i++) { mx = fmaxf(mx, sx[i]); my = fmaxf(my, sy[i]); }
    mx = fmaxf(mx, __shfl_xor(mx, 32)); my = fmaxf(my, __shfl_xor(my, 32));
  }
  f32x16 cx, cy, ox = {}, oy = {};
  for (int i = 0; i < 16; i++) { cx[i] = -mx; cy[i] = -my; }
  unsigned ka = lds0 + u * 32768 + lane * 16;
  unsigned va = lds0 + KIMG + u * 64 * VGS + ((lane & 16) ? 512 : (lane >> 5) * 256 + (lane & 15) * 16);
  unsigned idx = 0xdddd, cnt;
  asm volatile(
"ds_read_b128 a[40:43], %[ka] offset:0\n\t"
"ds_read_b128 a[48:51], %[va] offset:0\n\t"
"ds_read_b128 a[52:55], %[va] offset:528\n\t"
"v_accvgpr_write_b32 a60, 0\n\t"
"v_accvgpr_write_b32 a61, 0\n\t"
"v_accvgpr_write_b32 a62, 0\n\t"
"v_accvgpr_write_b32 a63, 0\n\t"
"v_mov_b32 v56, 0\n\t"
"v_mov_b32 v57, 0\n\t"
"v_mov_b32 v58, 0\n\t"
"v_mov_b32 v59, 0\n\t"
"v_mov_b32 v60, 0\n\t"
"v_mov_b32 v61, 0\n\t"
"v_mov_b32 v62, 0\n\t"
"v_mov_b32 v63, 0\n\t"
"s_waitcnt lgkmcnt(0)\n\t"
"s_nop 1\n\t"
"v_mfma_f32_32x32x16_bf16 v[32:47], a[40:43], a[32:35], v[64:79]\n\t"
"s_nop 7\n\t"
"s_nop 7\n\t"
"s_mov_b32 %[cnt], 8\n\t"
".p2align 6\n\t"
"1:\n\t"
"v_exp_f32 v32, v32\n\t"
"ds_read_b128 a[44:47], %[ka] offset:1024\n\t"
"v_exp_f32 v33, v33\n\t"
"v_smfmac_f32_32x32x32_bf16 a[16:31], a[60:63], v[56:63], %[idx]\n\t"
"v_exp_f32 v34, v34\n\t"
"v_exp_f32 v35, v35\n\t"
"v_exp_f32 v36, v36\n\t"
"ds_read_b128 a[56:59], %[va] offset:1056\n\t"
"v_exp_f32 v37, v37\n\t"
"v_mfma_f32_32x32x16_bf16 v[48:63], a[40:43], a[36:39], v[80:95]\n\t"
"v_exp_f32 v38, v38\n\t"
"v_exp_f32 v39, v39\n\t"
"v_exp_f32 v40, v40\n\t"
"v_exp_f32 v41, v41\n\t"
"v_smfmac_f32_32x32x32_bf16 a[0:15], a[48:51], v[32:39], %[idx]\n\t"
"v_exp_f32 v42, v42\n\t"
"v_exp_f32 v43, v43\n\t"
"v_exp_f32 v44, v44\n\t"
"ds_read_b128 a[60:63], %[va] offset:1584\n\t"
"v_exp_f32 v45, v45\n\t"
"v_exp_f32 v46, v46\n\t"
"v_exp_f32 v47, v47\n\t"
"v_exp_f32 v48, v48\n\t"
"v_exp_f32 v49, v49\n\t"
"v_smfmac_f32_32x32x32_bf16 a[0:15], a[52:55], v[40:47], %[idx]\n\t"
"v_exp_f32 v50, v50\n\t"
"v_exp_f32 v51, v51\n\t"
"v_exp_f32 v52, v52\n\t"
"v_exp_f32 v53, v53\n\t"
"s_waitcnt lgkmcnt(0)\n\t"
"v_mfma_f32_32x32x16_bf16 v[32:47], a[44:47], a[32:35], v[64:79]\n\t"
"v_exp_f32 v54, v54\n\t"
"v_exp_f32 v55, v55\n\t"
"v_exp_f32 v56, v56\n\t"
"v_exp_f32 v57, v57\n\t"
"v_smfmac_f32_32x32x32_bf16 a[16:31], a[48:51], v[48:55], %[idx]\n\t"
"v_exp_f32 v58, v58\n\t"
"v_exp_f32 v59, v59\n\t"
"v_exp_f32 v60, v60\n\t"
"v_exp_f32 v61, v61\n\t"
"v_exp_f32 v62, v62\n\t"
"v_exp_f32 v63, v63\n\t"
"v_exp_f32 v32, v32\n\t"
"ds_read_b128 a[40:43], %[ka] offset:2048\n\t"
"v_exp_f32 v33, v33\n\t"
"v_smfmac_f32_32x32x32_bf16 a[16:31], a[52:55], v[56:63], %[idx]\n\t"
"v_exp_f32 v34, v34\n\t"
"v_exp_f32 v35, v35\n\t"
"v_exp_f32 v36, v36\n\t"
"ds_read_b128 a[48:51], %[va] offset:2112\n\t"
"v_exp_f32 v37, v37\n\t"
"v_mfma_f32_32x32x16_bf16 v[48:63], a[44:47], a[36:39], v[80:95]\n\t"
"v_exp_f32 v38, v38\n\t"
"v_exp_f32 v39, v39\n\t"
"v_exp_f32 v40, v40\n\t"
"v_exp_f32 v41, v41\n\t"
"v_smfmac_f32_32x32x32_bf16 a[0:15], a[56:59], v[32:39], %[idx]\n\t"
"v_exp_f32 v42, v42\n\t"
"v_exp_f32 v43, v43\n\t"
"v_exp_f32 v44, v44\n\t"
"ds_read_b128 a[52:55], %[va] offset:2640\n\t"
"v_exp_f32 v45, v45\n\t"
"v_exp_f32 v46, v46\n\t"
"v_exp_f32 v47, v47\n\t"
"v_exp_f32 v48, v48\n\t"
"v_exp_f32 v49, v49\n\t"
"v_smfmac_f32_32x32x32_bf16 a[0:15], a[60:63], v[40:47], %[idx]\n\t"
"v_exp_f32 v50, v50\n\t"
"v_exp_f32 v51, v51\n\t"
"v_exp_f32 v52, v52\n\t"
"v_exp_f32 v53, v53\n\t"
"s_waitcnt lgkmcnt(0)\n\t"
"v_mfma_f32_32x32x16_bf16 v[32:47], a[40:43], a[32:35], v[64:79]\n\t"
"v_exp_f32 v54, v54\n\t"
"v_exp_f32 v55, v55\n\t"
"v_exp_f32 v56, v56\n\t"
"v_exp_f32 v57, v57\n\t"
"v_smfmac_f32_32x32x32_bf16 a[16:31], a[56:59], v[48:55], %[idx]\n\t"
"v_exp_f32 v58, v58\n\t"
"v_exp_f32 v59, v59\n\t"
"v_exp_f32 v60, v60\n\t"
"v_exp_f32 v61, v61\n\t"
"v_exp_f32 v62, v62\n\t"
"v_exp_f32 v63, v63\n\t"
"v_exp_f32 v32, v32\n\t"
"ds_read_b128 a[44:47], %[ka] offset:3072\n\t"
"v_exp_f32 v33, v33\n\t"
"v_smfmac_f32_32x32x32_bf16 a[16:31], a[60:63], v[56:63], %[idx]\n\t"
"v_exp_f32 v34, v34\n\t"
"v_exp_f32 v35, v35\n\t"
"v_exp_f32 v36, v36\n\t"
"ds_read_b128 a[56:59], %[va] offset:3168\n\t"
"v_exp_f32 v37, v37\n\t"
"v_mfma_f32_32x32x16_bf16 v[48:63], a[40:43], a[36:39], v[80:95]\n\t"
"v_exp_f32 v38, v38\n\t"
"v_exp_f32 v39, v39\n\t"
"v_exp_f32 v40, v40\n\t"
"v_exp_f32 v41, v41\n\t"
"v_smfmac_f32_32x32x32_bf16 a[0:15], a[48:51], v[32:39], %[idx]\n\t"
"v_exp_f32 v42, v42\n\t"
"v_exp_f32 v43, v43\n\t"
"v_exp_f32 v44, v44\n\t"
"ds_read_b128 a[60:63], %[va] offset:3696\n\t"
"v_exp_f32 v45, v45\n\t"
"v_exp_f32 v46, v46\n\t"
"v_exp_f32 v47, v47\n\t"
"v_exp_f32 v48, v48\n\t"
"v_exp_f32 v49, v49\n\t"
"v_smfmac_f32_32x32x32_bf16 a[0:15], a[52:55], v[40:47], %[idx]\n\t"
"v_exp_f32 v50, v50\n\t"
"v_exp_f32 v51, v51\n\t"
"v_exp_f32 v52, v52\n\t"
"v_exp_f32 v53, v53\n\t"
"s_waitcnt lgkmcnt(0)\n\t"
"v_mfma_f32_32x32x16_bf16 v[32:47], a[44:47], a[32:35], v[64:79]\n\t"
"v_exp_f32 v54, v54\n\t"
"v_exp_f32 v55, v55\n\t"
"v_exp_f32 v56, v56\n\t"
"v_exp_f32 v57, v57\n\t"
"v_smfmac_f32_32x32x32_bf16 a[16:31], a[48:51], v[48:55], %[idx]\n\t"
"v_exp_f32 v58, v58\n\t"
"v_exp_f32 v59, v59\n\t"
"v_exp_f32 v60, v60\n\t"
"v_exp_f32 v61, v61\n\t"
"v_exp_f32 v62, v62\n\t"
"v_exp_f32 v63, v63\n\t"
"v_exp_f32 v32, v32\n\t"
"ds_read_b128 a[40:43], %[ka] offset:4096\n\t"
"v_exp_f32 v33, v33\n\t"
"v_smfmac_f32_32x32x32_bf16 a[16:31], a[52:55], v[56:63], %[idx]\n\t"
"v_exp_f32 v34, v34\n\t"
"v_exp_f32 v35, v35\n\t"
"v_exp_f32 v36, v36\n\t"
"ds_read_b128 a[48:51], %[va] offset:4224\n\t"
"v_exp_f32 v37, v37\n\t"
"v_mfma_f32_32x32x16_bf16 v[48:63], a[44:47], a[36:39], v[80:95]\n\t"
"v_exp_f32 v38, v38\n\t"
"v_exp_f32 v39, v39\n\t"
"v_exp_f32 v40, v40\n\t"
"v_exp_f32 v41, v41\n\t"
"v_smfmac_f32_32x32x32_bf16 a[0:15], a[56:59], v[32:39], %[idx]\n\t"
"v_exp_f32 v42, v42\n\t"
"v_exp_f32 v43, v43\n\t"
"v_exp_f32 v44, v44\n\t"
"ds_read_b128 a[52:55], %[va] offset:4752\n\t"
"v_exp_f32 v45, v45\n\t"
"v_exp_f32 v46, v46\n\t"
"v_exp_f32 v47, v47\n\t"
"v_exp_f32 v48, v48\n\t"
"v_exp_f32 v49, v49\n\t"
"v_smfmac_f32_32x32x32_bf16 a[0:15], a[60:63], v[40:47], %[idx]\n\t"
"v_exp_f32 v50, v50\n\t"
"v_exp_f32 v51, v51\n\t"
"v_exp_f32 v52, v52\n\t"
"v_exp_f32 v53, v53\n\t"
"s_waitcnt lgkmcnt(0)\n\t"
"v_mfma_f32_32x32x16_bf16 v[32:47], a[40:43], a[32:35], v[64:79]\n\t"
"v_exp_f32 v54, v54\n\t"
"v_exp_f32 v55, v55\n\t"
"v_exp_f32 v56, v56\n\t"
"v_exp_f32 v57, v57\n\t"
"v_smfmac_f32_32x32x32_bf16 a[16:31], a[56:59], v[48:55], %[idx]\n\t"
"v_exp_f32 v58, v58\n\t"
"v_exp_f32 v59, v59\n\t"
"v_exp_f32 v60, v60\n\t"
"v_add_u32 %[ka], 4096, %[ka]\n\t"
"v_add_u32 %[va], 4224, %[va]\n\t"
"s_sub_u32 %[cnt], %[cnt], 1\n\t"
"s_cmp_lg_u32 %[cnt], 0\n\t"
"v_exp_f32 v61, v61\n\t"
"v_exp_f32 v62, v62\n\t"
"v_exp_f32 v63, v63\n\t"
"s_cbranch_scc1 1b\n\t"
"s_nop 1\n\t"
"v_smfmac_f32_32x32x32_bf16 a[16:31], a[60:63], v[56:63], %[idx]\n\t"
"s_nop 7\n\t"
"s_nop 7\n\t"
    : "+{a[0:15]}"(ox), "+{a[16:31]}"(oy), [ka] "+v"(ka), [va] "+v"(va), [cnt] "=&s"(cnt)
    : "{a[32:35]}"(qx), "{a[36:39]}"(qy), "{v[64:79]}"(cx), "{v[80:95]}"(cy), [idx] "v"(idx)
    : "v32","v33","v34","v35","v36","v37","v38","v39","v40","v41","v42","v43","v44","v45","v46","v47",
      "v48","v49","v50","v51","v52","v53","v54","v55","v56","v57","v58","v59","v60","v61","v62","v63",
      "a40","a41","a42","a43","a44","a45","a46","a47","a48","a49","a50","a51","a52","a53","a54","a55",
      "a56","a57","a58","a59","a60","a61","a62","a63","scc","memory");
  __syncthreads();
  LAS float* ex = (LAS float*)(lds0) + w * 9 * 64;
  for (int i = 0; i < 9; i++) ex[i * 64 + lane] = u ? ox[i] : oy[i];
  __syncthreads();
  LAS float* ey = (LAS float*)(lds0) + (w ^ 4) * 9 * 64;
  float o[9];
  for (int i = 0; i < 9; i++) o[i] = (u ? oy[i] : ox[i]) + ey[i * 64 + lane];
  float r = 1.0f / o[8];
  u32x4 of = {cvtpk(o[0] * r, o[1] * r), cvtpk(o[2] * r, o[3] * r), cvtpk(o[4] * r, o[5] * r), cvtpk(o[6] * r, o[7] * r)};
  Of[(rb * 32 + hd0 + u) * 64 + lane] = of;
}

__device__ __forceinline__ float phi(float t) { return (t > 0.f ? t + 1.f : __expf(t)) + 1e-6f; }
__global__ void a_kv(const float* qkv, float* kvz) {
  int hd = blockIdx.x, t = threadIdx.x; int d = t / 17, e = t % 17;
  float s = 0;
  for (int r = 0; r < S; r++) {
    float pk = bf2f(f2bf(phi(qkv[(size_t)r * 1536 + 512 + hd * 16 + d])));
    float v = e < 16 ? bf2f(f2bf(qkv[(size_t)r * 1536 + 1024 + hd * 16 + e])) : 1.f;
    s += pk * v;
  }
  kvz[hd * 272 + t] = s;
}
__global__ void a_o(const float* qkv, const float* kvz, u16* Oa) {
  int i = blockIdx.x * blockDim.x + threadIdx.x; int r = i >> 9, c = i & 511, hd = c >> 4, e = c & 15;
  float num = 0, den = 0;
  for (int d = 0; d < 16; d++) { float pq = phi(qkv[(size_t)r * 1536 + hd * 16 + d]); num += pq * kvz[hd * 272 + d * 17 + e]; den += pq * kvz[hd * 272 + d * 17 + 16]; }
  Oa[i] = f2bf(num / (den + 1e-6f));
}
__global__ void unlayoutO(const u16* Of, u16* Ob) {
  int i = blockIdx.x * blockDim.x + threadIdx.x;
  int j = i & 7, l = (i >> 3) & 63, hd = (i >> 9) & 31, rb = i >> 14;
  int d = 8 * (j >> 2) + 4 * (l >> 5) + (j & 3);
  Ob[(size_t)(rb * 32 + (l & 31)) * D + hd * 16 + d] = Of[i];
}
__global__ void tobf16(const float* c, u16* out) { int i = blockIdx.x * blockDim.x + threadIdx.x; out[i] = f2bf(c[i]); }

extern "C" void kernel_launch(void* const* d_in, const int* in_sizes, int n_in, void* d_out, int out_size, void* d_ws, size_t ws_size, hipStream_t stream) {
  const u16 *Ax = (const u16*)d_in[0], *Awqkv = (const u16*)d_in[1], *Awo = (const u16*)d_in[2];
  const u16 *Bx = (const u16*)d_in[3], *Bwq = (const u16*)d_in[4], *Bwk = (const u16*)d_in[5], *Bwv = (const u16*)d_in[6], *Bwo = (const u16*)d_in[7];
  char* ws = (char*)d_ws;
  float* qkvA = (float*)ws;
  float* qB = (float*)(ws + (16 << 20));
  float* kB = (float*)(ws + (21 << 20));
  float* vB = (float*)(ws + (22 << 20));
  u16* Qf = (u16*)(ws + (24 << 20));
  unsigned char* KV = (unsigned char*)(ws + (27 << 20));
  u16* Of = (u16*)(ws + (28 << 20));
  u16* Ob = (u16*)(ws + (31 << 20));
  u16* Oa = (u16*)(ws + (34 << 20));
  float* kvz = (float*)(ws + (37 << 20));
  float* outf = (float*)(ws + (40 << 20));
  gemm_naive<<<dim3(48, 64), 64, 0, stream>>>(Ax, Awqkv, qkvA, 1536, 1536);
  gemm_naive<<<dim3(16, 64), 64, 0, stream>>>(Bx, Bwq, qB, 512, 512);
  gemm_naive<<<dim3(1, 64), 64, 0, stream>>>(Bx, Bwk, kB, 16, 16);
  gemm_naive<<<dim3(1, 64), 64, 0, stream>>>(Bx, Bwv, vB, 16, 16);
  layoutB<<<S * D / 256, 256, 0, stream>>>(qB, kB, vB, Qf, KV);
  hipFuncSetAttribute((const void*)attn, hipFuncAttributeMaxDynamicSharedMemorySize, 140 * 1024);
  attn<<<256, 512, 140 * 1024, stream>>>((const u32x4*)Qf, KV, (u32x4*)Of);
  unlayoutO<<<S * D / 256, 256, 0, stream>>>(Of, Ob);
  a_kv<<<32, 272, 0, stream>>>(qkvA, kvz);
  a_o<<<S * D / 256, 256, 0, stream>>>(qkvA, kvz, Oa);
  gemm_naive<<<dim3(16, 64), 64, 0, stream>>>(Oa, Awo, outf, 512, 1024);
  gemm_naive<<<dim3(16, 64), 64, 0, stream>>>(Ob, Bwo, outf + 512, 512, 1024);
  tobf16<<<S * 1024 / 256, 256, 0, stream>>>(outf, (u16*)d_out);
}
```

```cpp
#include <hip/hip_runtime.h>
#include <hip/hip_bf16.h>
#include <stdint.h>

typedef float f32x16 __attribute__((ext_vector_type(16)));
typedef float f32x8 __attribute__((ext_vector_type(8)));
typedef float f32x4 __attribute__((ext_vector_type(4)));
typedef __bf16 bfv8 __attribute__((ext_vector_type(8)));
typedef __bf16 bfv16 __attribute__((ext_vector_type(16)));
typedef unsigned u32x4 __attribute__((ext_vector_type(4)));
typedef unsigned u32x2 __attribute__((ext_vector_type(2)));
typedef short s16x4 __attribute__((ext_vector_type(4)));
typedef unsigned short u16;
typedef unsigned long long u64;
typedef __amdgpu_buffer_rsrc_t rsrc_t;
#define LAS __attribute__((address_space(3)))
#define DI __device__ __forceinline__

constexpr float LOG2E = 1.4426950408889634f;
constexpr float QSCALE = 0.25f * LOG2E;
constexpr int SC1 = 16;
constexpr unsigned SPIN = 1u << 20;

constexpr unsigned WS_EPOCH = 0, WS_FKV = 4096, WS_FPART = 8192, WS_FRED = 16384, WS_FOA = 24576,
                   WS_KVF = 32768, WS_KV = 65536, WS_OA = 1u << 20, WS_OB = 3u << 20, WS_PART = 5u << 20, WS_BYTES = 9u << 20;
constexpr unsigned L_MISC = 131072, L_CNT = L_MISC, L_FLG = L_MISC + 1024, L_Q = L_MISC + 5120, L_KVP = L_MISC + 13312,
                   L_EX = L_MISC + 5120, LDS_BYTES = 163840;
constexpr int C_CH = 0, C_PR = 8, C_KVP = 12, C_GB = 13, C_WB = 14, C_Q = 16, C_X1 = 24, C_X2 = 32, C_X3 = 40, C_E = 48;

DI unsigned cvtpk(float lo, float hi) { unsigned r; asm("v_cvt_pk_bf16_f32 %0, %1, %2" : "=v"(r) : "v"(lo), "v"(hi)); return r; }
DI f32x16 mfma32(u32x4 a, u32x4 b, f32x16 c) {
  return __builtin_amdgcn_mfma_f32_32x32x16_bf16(__builtin_bit_cast(bfv8, a), __builtin_bit_cast(bfv8, b), c, 0, 0, 0); }
DI f32x4 mfma16(u32x4 a, u32x4 b, f32x4 c) {
  return __builtin_amdgcn_mfma_f32_16x16x32_bf16(__builtin_bit_cast(bfv8, a), __builtin_bit_cast(bfv8, b), c, 0, 0, 0); }
DI void dma(unsigned m0v, unsigned voff, rsrc_t r, unsigned soff) {
  asm volatile("s_mov_b32 m0, %0\n\ts_nop 0\n\tbuffer_load_dwordx4 %1, %2, %3 offen lds" :: "s"(m0v), "v"(voff), "s"(r), "s"(soff) : "m0", "memory"); }
DI void dma_sc1(unsigned m0v, unsigned voff, rsrc_t r, unsigned soff) {
  asm volatile("s_mov_b32 m0, %0\n\ts_nop 0\n\tbuffer_load_dwordx4 %1, %2, %3 offen sc1 lds" :: "s"(m0v), "v"(voff), "s"(r), "s"(soff) : "m0", "memory"); }
DI u32x4 ldsr128(unsigned a) { return *(const LAS u32x4*)a; }
DI void ldsw128(unsigned a, u32x4 v) { *(LAS u32x4*)a = v; }
DI u32x2 ldstr(unsigned a) { return __builtin_bit_cast(u32x2, __builtin_amdgcn_ds_read_tr16_b64_v4i16((LAS s16x4*)a)); }
DI u32x4 ldstr2(unsigned a, unsigned d) { u32x2 x = ldstr(a), y = ldstr(a + d); return u32x4{x[0], x[1], y[0], y[1]}; }
#define WAIT_VM(n) asm volatile("s_waitcnt vmcnt(" #n ")" ::: "memory")
DI void cnt_add(int c, unsigned v) {
  asm volatile("s_mov_b64 exec, 1\n\tds_add_u32 %0, %1\n\ts_mov_b64 exec, -1" :: "v"(L_CNT + 4 * c), "v"(v) : "memory"); }
DI unsigned cnt_add_rtn(int c) { unsigned t, s;
  asm volatile("s_mov_b64 exec, 1\n\tds_add_rtn_u32 %0, %2, %3\n\ts_waitcnt lgkmcnt(0)\n\tv_readfirstlane_b32 %1, %0\n\ts_mov_b64 exec, -1"
               : "=&v"(t), "=s"(s) : "v"(L_CNT + 4 * c), "v"(1u) : "memory"); return s; }
DI unsigned cnt_wait(unsigned addr, unsigned target) { unsigned t, s, sp;
  asm volatile("s_mov_b32 %2, 0x400000\n"
               "1:\n\tds_read_b32 %0, %3\n\ts_waitcnt lgkmcnt(0)\n\tv_readfirstlane_b32 %1, %0\n\ts_cmp_ge_u32 %1, %4\n\ts_cbranch_scc1 2f\n\t"
               "s_sleep 1\n\ts_sub_u32 %2, %2, 1\n\ts_cmp_lg_u32 %2, 0\n\ts_cbranch_scc1 1b\n2:"
               : "=&v"(t), "=&s"(s), "=&s"(sp) : "v"(addr), "s"(target) : "scc", "memory"); return s; }
DI u64 mix(u64 r) { u64 e = (r ^ (r >> 29)) * 0x9E3779B97F4A7C15ull + 0xD1B54A32D192ED03ull; return e == r ? e + 1 : e; }
DI void flag_wait(rsrc_t ws, unsigned voff, u32x2 e) {
  for (unsigned sp = SPIN; sp; --sp) {
    u32x2 f = __builtin_amdgcn_raw_buffer_load_b64(ws, voff, 0, SC1);
    if (__all(f[0] == e[0] && f[1] == e[1])) break;
    __builtin_amdgcn_s_sleep(4);
  }
  asm volatile("" ::: "memory");
}
DI void flag_set(rsrc_t ws, unsigned off, u32x2 e, int lane) {
  WAIT_VM(0);
  if (lane == 0) __builtin_amdgcn_raw_buffer_store_b64(e, ws, off, 0, SC1);
}
DI float phi(float t) { float e; asm("v_exp_f32_e64 %0, %1 clamp\n\ts_nop 0" : "=v"(e) : "v"(t * LOG2E)); return (fmaxf(t, 0.f) + 1e-6f) + e; }
DI u32x4 pack8(const f32x16& a, int o) { return u32x4{cvtpk(a[o], a[o + 1]), cvtpk(a[o + 2], a[o + 3]), cvtpk(a[o + 4], a[o + 5]), cvtpk(a[o + 6], a[o + 7])}; }
DI u32x4 nosent(u32x4 v) { return u32x4{min(v[0], 0xfffffffeu), min(v[1], 0xfffffffeu), min(v[2], 0xfffffffeu), min(v[3], 0xfffffffeu)}; }

#define ST(n) do {} while (0)

__global__ void __launch_bounds__(768, 3)
fused(const u16* __restrict__ Ax, const u16* __restrict__ Awqkv, const u16* __restrict__ Awo, const u16* __restrict__ Bx,
      const u16* __restrict__ Bwq, const u16* __restrict__ Bwk, const u16* __restrict__ Bwv, const u16* __restrict__ Bwo,
      u16* __restrict__ out, void* wsp) {
  const int lane = threadIdx.x & 63, wv = __builtin_amdgcn_readfirstlane(threadIdx.x >> 6);
  const int xcd = blockIdx.x & 7, rr = blockIdx.x >> 3;
  const int bi = (xcd >> 1) * 8 + (rr >> 2), bj = (xcd & 1) * 4 + (rr & 3);
  const rsrc_t ws = __builtin_amdgcn_make_buffer_rsrc(wsp, 0, WS_BYTES, 0x20000);
  const rsrc_t rBx = __builtin_amdgcn_make_buffer_rsrc((void*)Bx, 0, 2048 * 512 * 2, 0x20000);
  const bool isT = wv < 8;
  const int g4 = lane >> 4, q4 = (lane & 15) >> 2, p4 = lane & 3;
  ST(0);
  int rq, u0, u1;
  if (isT) {
    const int p = wv & 3, u = wv >> 2;
    u32x2 rv;
    asm volatile("buffer_load_dwordx2 %0, off, %1, 0 sc1" : "=v"(rv) : "s"(ws) : "memory");
    {
      const rsrc_t rw = __builtin_amdgcn_make_buffer_rsrc((void*)((bj & 1) ? Bwv : Bwk), 0, 512 * 16 * 2, 0x20000);
      const unsigned vx = (lane >> 3) * 1024 + (((lane & 7) ^ g4) << 4), so = (bi * 64 + (bj >> 1) * 16) * 1024 + wv * 128;
      dma(wv * 4096, vx, rBx, so); dma(wv * 4096 + 1024, vx ^ 64, rBx, so + 8192);
      dma(wv * 4096 + 2048, lane * 16, rw, wv * 2048); dma(wv * 4096 + 3072, lane * 16, rw, wv * 2048 + 1024);
    }
    if (wv == 0) *(LAS unsigned*)(L_CNT + lane * 4) = (lane == C_CH + 4) ? 4u : 0u;
    asm volatile("s_waitcnt lgkmcnt(0)\n\ts_barrier\n\ts_waitcnt vmcnt(0)" : "+v"(rv) :: "memory");
    ST(1);
    const u64 E = mix(((u64)(unsigned)__builtin_amdgcn_readfirstlane(rv[1]) << 32) | (unsigned)__builtin_amdgcn_readfirstlane(rv[0]));
    const u32x2 ev = {(unsigned)E, (unsigned)(E >> 32)};
    {
      const unsigned xa = wv * 4096 + (lane & 15) * 128 + ((g4 ^ ((lane & 15) >> 1)) << 4);
      const unsigned wa = wv * 4096 + 2048 + (8 * g4 + q4) * 32 + 8 * p4;
      u32x4 x0 = ldsr128(xa), x1 = ldsr128(xa ^ 64), w0 = ldstr2(wa, 128), w1 = ldstr2(wa + 1024, 128);
      f32x4 acc = {};
      if (bj & 1) { acc = mfma16(x0, w0, acc); acc = mfma16(x1, w1, acc); }
      else { acc = mfma16(w0, x0, acc); acc = mfma16(w1, x1, acc); }
      *(LAS f32x4*)(L_KVP + wv * 1024 + lane * 16) = acc;
      if (wv == 0 && lane == 0) *(LAS u32x2*)(L_CNT + 4 * C_E) = ev;
      if (cnt_add_rtn(C_KVP) == 7) {
        f32x4 s = *(const LAS f32x4*)(L_KVP + lane * 16);
#pragma unroll
        for (int i = 1; i < 8; i++) s += *(const LAS f32x4*)(L_KVP + i * 1024 + lane * 16);
        const unsigned o = (bj & 1) ? 2048 + (bj >> 1) * 512 + (g4 >> 1) * 256 + (lane & 15) * 16 + (g4 & 1) * 8
                                    : (bj >> 2) * 1024 + ((g4 & 1) * 32 + ((bj >> 1) & 1) * 16 + (lane & 15)) * 16 + (g4 >> 1) * 8;
        __builtin_amdgcn_raw_buffer_store_b64(u32x2{cvtpk(s[0], s[1]), cvtpk(s[2], s[3])}, ws, o, WS_KV + bi * 4096, SC1);
        flag_set(ws, WS_FKV + bi * 128 + bj * 8, ev, lane);
      }
    }
    ST(2);
    flag_wait(ws, WS_FKV + wv * 128 + (lane & 7) * 8, ev);
    ST(3);
#pragma unroll
    for (int i = 0; i < 4; i++) dma_sc1(wv * 4096 + i * 1024, lane * 16, ws, WS_KV + wv * 4096 + i * 1024);
    WAIT_VM(0);
    cnt_add(C_CH, 1);
    ST(4);
    cnt_wait(L_CNT + 4 * (C_Q + p), 1);
    const u32x4 qx = ldsr128(L_Q + p * 2048 + lane * 16), qy = ldsr128(L_Q + p * 2048 + 1024 + lane * 16);
    ST(5);
    cnt_wait(L_CNT + 4 * C_CH, 8);
    f32x16 cx, cy, ox = {}, oy = {};
    {
      const u32x4 k0 = ldsr128(lane * 16);
      const f32x16 sx = mfma32(k0, qx, f32x16{}), sy = mfma32(k0, qy, f32x16{});
      float mx = sx[0], my = sy[0];
#pragma unroll
      for (int i = 1; i < 16; i++) { mx = fmaxf(mx, sx[i]); my = fmaxf(my, sy[i]); }
      auto a = __builtin_amdgcn_permlane32_swap(__float_as_uint(mx), __float_as_uint(mx), false, false);
      auto b = __builtin_amdgcn_permlane32_swap(__float_as_uint(my), __float_as_uint(my), false, false);
      mx = -fmaxf(__uint_as_float(a[0]), __uint_as_float(a[1])); my = -fmaxf(__uint_as_float(b[0]), __uint_as_float(b[1]));
#pragma unroll
      for (int i = 0; i < 16; i++) { cx[i] = mx; cy[i] = my; }
    }
    const unsigned vinit = ((lane & 27) == 16) ? 0x3f803f80u : 0u;
    ST(6);
    {
      unsigned ka = u * 16384 + lane * 16, va = u * 16384 + 2048 + (lane >> 5) * 256 + (lane & 15) * 16, ca = L_CNT;
      unsigned cnt, st, sp, vt;
      asm volatile(
"v_accvgpr_write_b32 a48, %[vinit]\n\t"
"v_accvgpr_write_b32 a49, %[vinit]\n\t"
"v_accvgpr_write_b32 a50, %[vinit]\n\t"
"v_accvgpr_write_b32 a51, %[vinit]\n\t"
"v_accvgpr_write_b32 a52, %[vinit]\n\t"
"v_accvgpr_write_b32 a53, %[vinit]\n\t"
"v_accvgpr_write_b32 a54, %[vinit]\n\t"
"v_accvgpr_write_b32 a55, %[vinit]\n\t"
"v_accvgpr_write_b32 a56, %[vinit]\n\t"
"v_accvgpr_write_b32 a57, %[vinit]\n\t"
"v_accvgpr_write_b32 a58, %[vinit]\n\t"
"v_accvgpr_write_b32 a59, %[vinit]\n\t"
"v_accvgpr_write_b32 a60, %[vinit]\n\t"
"v_accvgpr_write_b32 a61, %[vinit]\n\t"
"v_accvgpr_write_b32 a62, %[vinit]\n\t"
"v_accvgpr_write_b32 a63, %[vinit]\n\t"
"ds_read_b128 a[40:43], %[ka] offset:0\n\t"
"s_mov_b64 exec, %[vm]\n\t"
"ds_read_b128 a[48:51], %[va] offset:0\n\t"
"ds_read_b128 a[52:55], %[va] offset:512\n\t"
"s_mov_b64 exec, -1\n\t"
"v_mov_b32 v40, 0\n\t"
"v_mov_b32 v41, 0\n\t"
"v_mov_b32 v42, 0\n\t"
"v_mov_b32 v43, 0\n\t"
"v_mov_b32 v44, 0\n\t"
"v_mov_b32 v45, 0\n\t"
"v_mov_b32 v46, 0\n\t"
"v_mov_b32 v47, 0\n\t"
"s_mov_b32 %[cnt], 4\n\t"
"s_setprio 2\n\t"
"s_waitcnt lgkmcnt(0)\n\t"
"v_mfma_f32_32x32x16_bf16 v[16:31], a[40:43], a[32:35], v[48:63]\n\t"
"s_nop 7\n\t"
"s_nop 7\n\t"
".p2align 6\n\t"
"1:\n\t"
"v_exp_f32 v16, v16\n\t"
"ds_read_b128 a[44:47], %[ka] offset:1024\n\t"
"v_exp_f32 v17, v17\n\t"
"v_smfmac_f32_32x32x32_bf16 a[16:31], a[60:63], v[40:47], %[idx]\n\t"
"v_exp_f32 v18, v18\n\t"
"v_exp_f32 v19, v19\n\t"
"v_exp_f32 v20, v20\n\t"
"s_mov_b64 exec, %[vm]\n\t"
"ds_read_b128 a[56:59], %[va] offset:1024\n\t"
"ds_read_b128 a[60:63], %[va] offset:1536\n\t"
"s_mov_b64 exec, -1\n\t"
"v_exp_f32 v21, v21\n\t"
"v_mfma_f32_32x32x16_bf16 v[32:47], a[40:43], a[36:39], v[64:79]\n\t"
"v_exp_f32 v22, v22\n\t"
"v_exp_f32 v23, v23\n\t"
"v_exp_f32 v24, v24\n\t"
"v_exp_f32 v25, v25\n\t"
"v_smfmac_f32_32x32x32_bf16 a[0:15], a[48:51], v[16:23], %[idx]\n\t"
"v_exp_f32 v26, v26\n\t"
"v_exp_f32 v27, v27\n\t"
"v_exp_f32 v28, v28\n\t"
"v_exp_f32 v29, v29\n\t"
"v_exp_f32 v30, v30\n\t"
"v_exp_f32 v31, v31\n\t"
"v_exp_f32 v32, v32\n\t"
"v_exp_f32 v33, v33\n\t"
"v_smfmac_f32_32x32x32_bf16 a[0:15], a[52:55], v[24:31], %[idx]\n\t"
"v_exp_f32 v34, v34\n\t"
"v_exp_f32 v35, v35\n\t"
"v_exp_f32 v36, v36\n\t"
"v_exp_f32 v37, v37\n\t"
"s_waitcnt lgkmcnt(0)\n\t"
"v_mfma_f32_32x32x16_bf16 v[16:31], a[44:47], a[32:35], v[48:63]\n\t"
"v_exp_f32 v38, v38\n\t"
"v_exp_f32 v39, v39\n\t"
"v_exp_f32 v40, v40\n\t"
"v_exp_f32 v41, v41\n\t"
"v_smfmac_f32_32x32x32_bf16 a[16:31], a[48:51], v[32:39], %[idx]\n\t"
"v_exp_f32 v42, v42\n\t"
"v_exp_f32 v43, v43\n\t"
"v_exp_f32 v44, v44\n\t"
"v_exp_f32 v45, v45\n\t"
"v_exp_f32 v46, v46\n\t"
"v_exp_f32 v47, v47\n\t"
"v_exp_f32 v16, v16\n\t"
"ds_read_b128 a[40:43], %[ka] offset:4096\n\t"
"v_exp_f32 v17, v17\n\t"
"v_smfmac_f32_32x32x32_bf16 a[16:31], a[52:55], v[40:47], %[idx]\n\t"
"v_exp_f32 v18, v18\n\t"
"v_exp_f32 v19, v19\n\t"
"v_exp_f32 v20, v20\n\t"
"s_mov_b64 exec, %[vm]\n\t"
"ds_read_b128 a[48:51], %[va] offset:4096\n\t"
"ds_read_b128 a[52:55], %[va] offset:4608\n\t"
"s_mov_b64 exec, -1\n\t"
"v_exp_f32 v21, v21\n\t"
"v_mfma_f32_32x32x16_bf16 v[32:47], a[44:47], a[36:39], v[64:79]\n\t"
"v_exp_f32 v22, v22\n\t"
"v_exp_f32 v23, v23\n\t"
"v_exp_f32 v24, v24\n\t"
"v_exp_f32 v25, v25\n\t"
"v_smfmac_f32_32x32x32_bf16 a[0:15], a[56:59], v[16:23], %[idx]\n\t"
"v_exp_f32 v26, v26\n\t"
"v_exp_f32 v27, v27\n\t"
"v_exp_f32 v28, v28\n\t"
"v_exp_f32 v29, v29\n\t"
"v_exp_f32 v30, v30\n\t"
"v_exp_f32 v31, v31\n\t"
"v_exp_f32 v32, v32\n\t"
"v_exp_f32 v33, v33\n\t"
"v_smfmac_f32_32x32x32_bf16 a[0:15], a[60:63], v[24:31], %[idx]\n\t"
"v_exp_f32 v34, v34\n\t"
"v_exp_f32 v35, v35\n\t"
"v_exp_f32 v36, v36\n\t"
"v_exp_f32 v37, v37\n\t"
"s_waitcnt lgkmcnt(0)\n\t"
"v_mfma_f32_32x32x16_bf16 v[16:31], a[40:43], a[32:35], v[48:63]\n\t"
"v_exp_f32 v38, v38\n\t"
"v_exp_f32 v39, v39\n\t"
"v_exp_f32 v40, v40\n\t"
"v_exp_f32 v41, v41\n\t"
"v_smfmac_f32_32x32x32_bf16 a[16:31], a[56:59], v[32:39], %[idx]\n\t"
"v_exp_f32 v42, v42\n\t"
"v_exp_f32 v43, v43\n\t"
"v_exp_f32 v44, v44\n\t"
"v_exp_f32 v45, v45\n\t"
"v_exp_f32 v46, v46\n\t"
"v_exp_f32 v47, v47\n\t"
"v_exp_f32 v16, v16\n\t"
"ds_read_b128 a[44:47], %[ka] offset:5120\n\t"
"v_exp_f32 v17, v17\n\t"
"v_smfmac_f32_32x32x32_bf16 a[16:31], a[60:63], v[40:47], %[idx]\n\t"
"v_exp_f32 v18, v18\n\t"
"v_exp_f32 v19, v19\n\t"
"v_exp_f32 v20, v20\n\t"
"s_mov_b64 exec, %[vm]\n\t"
"ds_read_b128 a[56:59], %[va] offset:5120\n\t"
"ds_read_b128 a[60:63], %[va] offset:5632\n\t"
"s_mov_b64 exec, -1\n\t"
"v_exp_f32 v21, v21\n\t"
"v_mfma_f32_32x32x16_bf16 v[32:47], a[40:43], a[36:39], v[64:79]\n\t"
"v_exp_f32 v22, v22\n\t"
"v_exp_f32 v23, v23\n\t"
"v_exp_f32 v24, v24\n\t"
"v_exp_f32 v25, v25\n\t"
"v_smfmac_f32_32x32x32_bf16 a[0:15], a[48:51], v[16:23], %[idx]\n\t"
"v_exp_f32 v26, v26\n\t"
"v_exp_f32 v27, v27\n\t"
"v_exp_f32 v28, v28\n\t"
"v_exp_f32 v29, v29\n\t"
"v_exp_f32 v30, v30\n\t"
"v_exp_f32 v31, v31\n\t"
"v_exp_f32 v32, v32\n\t"
"v_exp_f32 v33, v33\n\t"
"v_smfmac_f32_32x32x32_bf16 a[0:15], a[52:55], v[24:31], %[idx]\n\t"
"v_exp_f32 v34, v34\n\t"
"v_exp_f32 v35, v35\n\t"
"v_exp_f32 v36, v36\n\t"
"v_exp_f32 v37, v37\n\t"
"s_waitcnt lgkmcnt(0)\n\t"
"v_mfma_f32_32x32x16_bf16 v[16:31], a[44:47], a[32:35], v[48:63]\n\t"
"v_exp_f32 v38, v38\n\t"
"v_exp_f32 v39, v39\n\t"
"v_exp_f32 v40, v40\n\t"
"v_exp_f32 v41, v41\n\t"
"v_smfmac_f32_32x32x32_bf16 a[16:31], a[48:51], v[32:39], %[idx]\n\t"
"v_exp_f32 v42, v42\n\t"
"v_exp_f32 v43, v43\n\t"
"v_exp_f32 v44, v44\n\t"
"v_exp_f32 v45, v45\n\t"
"v_exp_f32 v46, v46\n\t"
"v_exp_f32 v47, v47\n\t"
"v_exp_f32 v16, v16\n\t"
"ds_read_b128 a[40:43], %[ka] offset:8192\n\t"
"v_exp_f32 v17, v17\n\t"
"v_smfmac_f32_32x32x32_bf16 a[16:31], a[52:55], v[40:47], %[idx]\n\t"
"v_exp_f32 v18, v18\n\t"
"v_exp_f32 v19, v19\n\t"
"v_exp_f32 v20, v20\n\t"
"s_mov_b64 exec, %[vm]\n\t"
"ds_read_b128 a[48:51], %[va] offset:8192\n\t"
"ds_read_b128 a[52:55], %[va] offset:8704\n\t"
"s_mov_b64 exec, -1\n\t"
"v_exp_f32 v21, v21\n\t"
"v_mfma_f32_32x32x16_bf16 v[32:47], a[44:47], a[36:39], v[64:79]\n\t"
"v_exp_f32 v22, v22\n\t"
"v_exp_f32 v23, v23\n\t"
"v_exp_f32 v24, v24\n\t"
"v_exp_f32 v25, v25\n\t"
"v_smfmac_f32_32x32x32_bf16 a[0:15], a[56:59], v[16:23], %[idx]\n\t"
"v_exp_f32 v26, v26\n\t"
"v_exp_f32 v27, v27\n\t"
"v_exp_f32 v28, v28\n\t"
"v_exp_f32 v29, v29\n\t"
"v_exp_f32 v30, v30\n\t"
"v_exp_f32 v31, v31\n\t"
"v_exp_f32 v32, v32\n\t"
"v_exp_f32 v33, v33\n\t"
"v_smfmac_f32_32x32x32_bf16 a[0:15], a[60:63], v[24:31], %[idx]\n\t"
"v_exp_f32 v34, v34\n\t"
"v_exp_f32 v35, v35\n\t"
"v_exp_f32 v36, v36\n\t"
"v_exp_f32 v37, v37\n\t"
"s_waitcnt lgkmcnt(0)\n\t"
"v_mfma_f32_32x32x16_bf16 v[16:31], a[40:43], a[32:35], v[48:63]\n\t"
"v_exp_f32 v38, v38\n\t"
"v_exp_f32 v39, v39\n\t"
"v_exp_f32 v40, v40\n\t"
"v_exp_f32 v41, v41\n\t"
"v_smfmac_f32_32x32x32_bf16 a[16:31], a[56:59], v[32:39], %[idx]\n\t"
"v_exp_f32 v42, v42\n\t"
"v_exp_f32 v43, v43\n\t"
"v_exp_f32 v44, v44\n\t"
"v_exp_f32 v45, v45\n\t"
"v_exp_f32 v46, v46\n\t"
"v_exp_f32 v47, v47\n\t"
"v_exp_f32 v16, v16\n\t"
"ds_read_b128 a[44:47], %[ka] offset:9216\n\t"
"v_exp_f32 v17, v17\n\t"
"v_smfmac_f32_32x32x32_bf16 a[16:31], a[60:63], v[40:47], %[idx]\n\t"
"v_exp_f32 v18, v18\n\t"
"v_exp_f32 v19, v19\n\t"
"v_exp_f32 v20, v20\n\t"
"s_mov_b64 exec, %[vm]\n\t"
"ds_read_b128 a[56:59], %[va] offset:9216\n\t"
"ds_read_b128 a[60:63], %[va] offset:9728\n\t"
"s_mov_b64 exec, -1\n\t"
"v_exp_f32 v21, v21\n\t"
"v_mfma_f32_32x32x16_bf16 v[32:47], a[40:43], a[36:39], v[64:79]\n\t"
"v_exp_f32 v22, v22\n\t"
"v_exp_f32 v23, v23\n\t"
"v_exp_f32 v24, v24\n\t"
"v_exp_f32 v25, v25\n\t"
"v_smfmac_f32_32x32x32_bf16 a[0:15], a[48:51], v[16:23], %[idx]\n\t"
"v_exp_f32 v26, v26\n\t"
"v_exp_f32 v27, v27\n\t"
"v_exp_f32 v28, v28\n\t"
"v_exp_f32 v29, v29\n\t"
"v_exp_f32 v30, v30\n\t"
"v_exp_f32 v31, v31\n\t"
"v_exp_f32 v32, v32\n\t"
"v_exp_f32 v33, v33\n\t"
"v_smfmac_f32_32x32x32_bf16 a[0:15], a[52:55], v[24:31], %[idx]\n\t"
"v_exp_f32 v34, v34\n\t"
"v_exp_f32 v35, v35\n\t"
"v_exp_f32 v36, v36\n\t"
"v_exp_f32 v37, v37\n\t"
"s_waitcnt lgkmcnt(0)\n\t"
"v_mfma_f32_32x32x16_bf16 v[16:31], a[44:47], a[32:35], v[48:63]\n\t"
"v_exp_f32 v38, v38\n\t"
"v_exp_f32 v39, v39\n\t"
"v_exp_f32 v40, v40\n\t"
"v_exp_f32 v41, v41\n\t"
"v_smfmac_f32_32x32x32_bf16 a[16:31], a[48:51], v[32:39], %[idx]\n\t"
"v_exp_f32 v42, v42\n\t"
"v_exp_f32 v43, v43\n\t"
"v_exp_f32 v44, v44\n\t"
"v_exp_f32 v45, v45\n\t"
"v_exp_f32 v46, v46\n\t"
"v_exp_f32 v47, v47\n\t"
"v_exp_f32 v16, v16\n\t"
"ds_read_b128 a[40:43], %[ka] offset:12288\n\t"
"v_exp_f32 v17, v17\n\t"
"v_smfmac_f32_32x32x32_bf16 a[16:31], a[52:55], v[40:47], %[idx]\n\t"
"v_exp_f32 v18, v18\n\t"
"v_exp_f32 v19, v19\n\t"
"v_exp_f32 v20, v20\n\t"
"s_mov_b64 exec, %[vm]\n\t"
"ds_read_b128 a[48:51], %[va] offset:12288\n\t"
"ds_read_b128 a[52:55], %[va] offset:12800\n\t"
"s_mov_b64 exec, -1\n\t"
"v_exp_f32 v21, v21\n\t"
"v_mfma_f32_32x32x16_bf16 v[32:47], a[44:47], a[36:39], v[64:79]\n\t"
"v_exp_f32 v22, v22\n\t"
"v_exp_f32 v23, v23\n\t"
"v_exp_f32 v24, v24\n\t"
"v_exp_f32 v25, v25\n\t"
"v_smfmac_f32_32x32x32_bf16 a[0:15], a[56:59], v[16:23], %[idx]\n\t"
"v_exp_f32 v26, v26\n\t"
"v_exp_f32 v27, v27\n\t"
"v_exp_f32 v28, v28\n\t"
"v_exp_f32 v29, v29\n\t"
"v_exp_f32 v30, v30\n\t"
"v_exp_f32 v31, v31\n\t"
"v_exp_f32 v32, v32\n\t"
"v_exp_f32 v33, v33\n\t"
"v_smfmac_f32_32x32x32_bf16 a[0:15], a[60:63], v[24:31], %[idx]\n\t"
"v_exp_f32 v34, v34\n\t"
"v_exp_f32 v35, v35\n\t"
"v_exp_f32 v36, v36\n\t"
"v_exp_f32 v37, v37\n\t"
"s_waitcnt lgkmcnt(0)\n\t"
"v_mfma_f32_32x32x16_bf16 v[16:31], a[40:43], a[32:35], v[48:63]\n\t"
"v_exp_f32 v38, v38\n\t"
"v_exp_f32 v39, v39\n\t"
"v_exp_f32 v40, v40\n\t"
"v_exp_f32 v41, v41\n\t"
"v_smfmac_f32_32x32x32_bf16 a[16:31], a[56:59], v[32:39], %[idx]\n\t"
"v_exp_f32 v42, v42\n\t"
"v_exp_f32 v43, v43\n\t"
"v_exp_f32 v44, v44\n\t"
"v_exp_f32 v45, v45\n\t"
"v_exp_f32 v46, v46\n\t"
"v_exp_f32 v47, v47\n\t"
"v_exp_f32 v16, v16\n\t"
"ds_read_b128 a[44:47], %[ka] offset:13312\n\t"
"v_exp_f32 v17, v17\n\t"
"v_smfmac_f32_32x32x32_bf16 a[16:31], a[60:63], v[40:47], %[idx]\n\t"
"v_exp_f32 v18, v18\n\t"
"v_exp_f32 v19, v19\n\t"
"v_exp_f32 v20, v20\n\t"
"s_mov_b64 exec, %[vm]\n\t"
"ds_read_b128 a[56:59], %[va] offset:13312\n\t"
"ds_read_b128 a[60:63], %[va] offset:13824\n\t"
"s_mov_b64 exec, -1\n\t"
"v_exp_f32 v21, v21\n\t"
"v_mfma_f32_32x32x16_bf16 v[32:47], a[40:43], a[36:39], v[64:79]\n\t"
"v_exp_f32 v22, v22\n\t"
"v_exp_f32 v23, v23\n\t"
"v_exp_f32 v24, v24\n\t"
"v_exp_f32 v25, v25\n\t"
"v_smfmac_f32_32x32x32_bf16 a[0:15], a[48:51], v[16:23], %[idx]\n\t"
"v_exp_f32 v26, v26\n\t"
"v_exp_f32 v27, v27\n\t"
"v_exp_f32 v28, v28\n\t"
"ds_read_b32 %[vt], %[ca] offset:4\n\t"
"v_exp_f32 v29, v29\n\t"
"v_exp_f32 v30, v30\n\t"
"v_exp_f32 v31, v31\n\t"
"v_exp_f32 v32, v32\n\t"
"v_exp_f32 v33, v33\n\t"
"v_smfmac_f32_32x32x32_bf16 a[0:15], a[52:55], v[24:31], %[idx]\n\t"
"v_exp_f32 v34, v34\n\t"
"v_exp_f32 v35, v35\n\t"
"v_exp_f32 v36, v36\n\t"
"v_exp_f32 v37, v37\n\t"
"s_waitcnt lgkmcnt(0)\n\t"
"v_mfma_f32_32x32x16_bf16 v[16:31], a[44:47], a[32:35], v[48:63]\n\t"
"v_exp_f32 v38, v38\n\t"
"v_exp_f32 v39, v39\n\t"
"v_exp_f32 v40, v40\n\t"
"v_exp_f32 v41, v41\n\t"
"v_smfmac_f32_32x32x32_bf16 a[16:31], a[48:51], v[32:39], %[idx]\n\t"
"v_exp_f32 v42, v42\n\t"
"v_exp_f32 v43, v43\n\t"
"v_exp_f32 v44, v44\n\t"
"v_exp_f32 v45, v45\n\t"
"v_exp_f32 v46, v46\n\t"
"v_exp_f32 v47, v47\n\t"
"v_readfirstlane_b32 %[st], %[vt]\n\t"
"s_cmp_ge_u32 %[st], 4\n\t"
"s_cbranch_scc0 3f\n\t"
"4:\n\t"
"v_exp_f32 v16, v16\n\t"
"ds_read_b128 a[40:43], %[ka] offset:32768\n\t"
"v_exp_f32 v17, v17\n\t"
"v_smfmac_f32_32x32x32_bf16 a[16:31], a[52:55], v[40:47], %[idx]\n\t"
"v_exp_f32 v18, v18\n\t"
"v_exp_f32 v19, v19\n\t"
"v_exp_f32 v20, v20\n\t"
"s_mov_b64 exec, %[vm]\n\t"
"ds_read_b128 a[48:51], %[va] offset:32768\n\t"
"ds_read_b128 a[52:55], %[va] offset:33280\n\t"
"s_mov_b64 exec, -1\n\t"
"v_exp_f32 v21, v21\n\t"
"v_mfma_f32_32x32x16_bf16 v[32:47], a[44:47], a[36:39], v[64:79]\n\t"
"v_exp_f32 v22, v22\n\t"
"v_exp_f32 v23, v23\n\t"
"v_exp_f32 v24, v24\n\t"
"v_exp_f32 v25, v25\n\t"
"v_smfmac_f32_32x32x32_bf16 a[0:15], a[56:59], v[16:23], %[idx]\n\t"
"v_exp_f32 v26, v26\n\t"
"v_exp_f32 v27, v27\n\t"
"v_exp_f32 v28, v28\n\t"
"ds_add_u32 %[ca], %[one] offset:32\n\t"
"v_exp_f32 v29, v29\n\t"
"v_exp_f32 v30, v30\n\t"
"v_exp_f32 v31, v31\n\t"
"v_exp_f32 v32, v32\n\t"
"v_exp_f32 v33, v33\n\t"
"v_smfmac_f32_32x32x32_bf16 a[0:15], a[60:63], v[24:31], %[idx]\n\t"
"v_exp_f32 v34, v34\n\t"
"v_exp_f32 v35, v35\n\t"
"v_exp_f32 v36, v36\n\t"
"v_exp_f32 v37, v37\n\t"
"s_waitcnt lgkmcnt(0)\n\t"
"v_mfma_f32_32x32x16_bf16 v[16:31], a[40:43], a[32:35], v[48:63]\n\t"
"v_exp_f32 v38, v38\n\t"
"v_exp_f32 v39, v39\n\t"
"v_exp_f32 v40, v40\n\t"
"v_exp_f32 v41, v41\n\t"
"v_smfmac_f32_32x32x32_bf16 a[16:31], a[56:59], v[32:39], %[idx]\n\t"
"v_exp_f32 v42, v42\n\t"
"v_exp_f32 v43, v43\n\t"
"v_add_u32 %[ka], 0x8000, %[ka]\n\t"
"v_add_u32 %[va], 0x8000, %[va]\n\t"
"v_add_u32 %[ca], 4, %[ca]\n\t"
"s_sub_u32 %[cnt], %[cnt], 1\n\t"
"s_cmp_lg_u32 %[cnt], 2\n\t"
"s_cbranch_scc1 5f\n\t"
"s_setprio 1\n\t"
"5:\n\t"
"s_cmp_lg_u32 %[cnt], 1\n\t"
"s_cbranch_scc1 6f\n\t"
"s_setprio 0\n\t"
"6:\n\t"
"s_cmp_lg_u32 %[cnt], 0\n\t"
"v_exp_f32 v44, v44\n\t"
"v_exp_f32 v45, v45\n\t"
"v_exp_f32 v46, v46\n\t"
"v_exp_f32 v47, v47\n\t"
"s_cbranch_scc1 1b\n\t"
"s_nop 1\n\t"
"v_smfmac_f32_32x32x32_bf16 a[16:31], a[60:63], v[40:47], %[idx]\n\t"
"s_branch 7f\n\t"
"3:\n\t"
"s_mov_b32 %[sp], 0x400000\n\t"
"8:\n\t"
"s_sleep 1\n\t"
"ds_read_b32 %[vt], %[ca] offset:4\n\t"
"s_waitcnt lgkmcnt(0)\n\t"
"v_readfirstlane_b32 %[st], %[vt]\n\t"
"s_cmp_ge_u32 %[st], 4\n\t"
"s_cbranch_scc1 4b\n\t"
"s_sub_u32 %[sp], %[sp], 1\n\t"
"s_cmp_lg_u32 %[sp], 0\n\t"
"s_cbranch_scc1 8b\n\t"
"s_branch 4b\n\t"
"7:\n\t"
"s_nop 7\n\t"
"s_nop 7\n\t"
        : "+{a[0:15]}"(ox), "+{a[16:31]}"(oy), [ka] "+v"(ka), [va] "+v"(va), [ca] "+v"(ca), [cnt] "=&s"(cnt), [st] "=&s"(st), [sp] "=&s"(sp), [vt] "=&v"(vt)
        : "{a[32:35]}"(qx), "{a[36:39]}"(qy), "{v[48:63]}"(cx), "{v[64:79]}"(cy), [idx] "v"(0xddddu), [vm] "s"(0x0000ffff0000ffffull),
          [vinit] "v"(vinit), [one] "v"(1u)
        : "v16","v17","v18","v19","v20","v21","v22","v23","v24","v25","v26","v27","v28","v29","v30","v31",
          "v32","v33","v34","v35","v36","v37","v38","v39","v40","v41","v42","v43","v44","v45","v46","v47",
          "a40","a41","a42","a43","a44","a45","a46","a47","a48","a49","a50","a51","a52","a53","a54","a55",
          "a56","a57","a58","a59","a60","a61","a62","a63","scc","memory");
    }
    ST(7);
    float o[9];
    {
      float chk = 0.f;
#pragma unroll
      for (int i = 0; i < 9; i++) { chk = fmaxf(chk, fabsf(ox[i])); chk = fmaxf(chk, fabsf(oy[i])); }
      float nanp = 0.f;
#pragma unroll
      for (int i = 0; i < 9; i++) nanp += ox[i] + oy[i];
      const bool bad = __any(!(chk <= 1.0e18f) || nanp != nanp);
#pragma unroll
      for (int i = 0; i < 9; i++) *(LAS float*)(L_EX + wv * 2304 + i * 256 + lane * 4) = u ? ox[i] : oy[i];
      cnt_add(C_X1 + wv, bad ? 2 : 1);
      const unsigned pb = cnt_wait(L_CNT + 4 * (C_X1 + (wv ^ 4)), 1);
#pragma unroll
      for (int i = 0; i < 9; i++) o[i] = (u ? oy[i] : ox[i]) + *(const LAS float*)(L_EX + (wv ^ 4) * 2304 + i * 256 + lane * 4);
      if (__builtin_expect(bad || pb == 2, 0)) {
        const u32x4 qq = u ? qy : qx;
        float m = -INFINITY;
        for (int t = 0; t < 64; t++) {
          const u32x4 kf = __builtin_amdgcn_raw_buffer_load_b128(ws, lane * 16, WS_KV + (t >> 1) * 4096 + (t & 1) * 1024, SC1);
          const f32x16 s = mfma32(kf, qq, f32x16{});
#pragma unroll
          for (int i = 0; i < 16; i++) m = fmaxf(m, s[i]);
        }
        m = fmaxf(m, __shfl_xor(m, 32));
        f32x16 acc = {};
        for (int t = 0; t < 64; t++) {
          const unsigned so = WS_KV + (t >> 1) * 4096 + (t & 1) * 1024;
          const u32x4 kf = __builtin_amdgcn_raw_buffer_load_b128(ws, lane * 16, so, SC1);
          f32x16 s = mfma32(kf, qq, f32x16{});
#pragma unroll
          for (int h = 0; h < 2; h++) {
            u32x4 vf = __builtin_amdgcn_raw_buffer_load_b128(ws, (lane >> 5) * 256 + (lane & 15) * 16, so + 2048 + h * 512, SC1);
            if (lane & 16) vf = u32x4{vinit, vinit, vinit, vinit};
            f32x8 pp;
#pragma unroll
            for (int i = 0; i < 8; i++) pp[i] = __builtin_amdgcn_exp2f(s[8 * h + i] - m);
            acc = __builtin_amdgcn_smfmac_f32_32x32x32_bf16(__builtin_bit_cast(bfv8, vf), __builtin_bit_cast(bfv16, pp), acc, 0xdddd, 0, 0);
          }
        }
#pragma unroll
        for (int i = 0; i < 9; i++) o[i] = acc[i];
      }
    }
    cnt_wait(L_CNT + 4 * C_WB, 4);
    {
      const float r = __builtin_amdgcn_rcpf(o[8]);
      const u32x4 of = nosent(u32x4{cvtpk(o[0] * r, o[1] * r), cvtpk(o[2] * r, o[3] * r), cvtpk(o[4] * r, o[5] * r), cvtpk(o[6] * r, o[7] * r)});
      const unsigned vo = ((lane >> 4) & 1) * 16384 + (lane >> 5) * 256 + (lane & 15) * 16;
      __builtin_amdgcn_raw_buffer_store_b128(of, ws, vo, WS_OB + ((bi * 4 + (p & 1) * 2) * 16 + bj * 2 + (p >> 1)) * 1024 + u * 512, SC1);
    }
    ST(8);
    rq = p; u0 = u; u1 = u + 1;
  } else {
    const int g = wv - 8, rh = g & 1, ch = g >> 1, jc = bj * 2 + ch, rho = bi * 2 + rh;
    __builtin_amdgcn_s_setprio(3);
    const rsrc_t rBq = __builtin_amdgcn_make_buffer_rsrc((void*)Bwq, 0, 512 * 512 * 2, 0x20000);
    const rsrc_t rAx = __builtin_amdgcn_make_buffer_rsrc((void*)Ax, 0, 2048 * 512 * 2, 0x20000);
    const rsrc_t rAw = __builtin_amdgcn_make_buffer_rsrc((void*)Awqkv, 0, 512 * 1536 * 2, 0x20000);
    const unsigned vx = (lane >> 3) * 1024 + (((lane & 7) ^ (4 * (g & 1) + g4)) << 4);
    const unsigned vwc = ((lane & 7) ^ ((g4 & 1) << 2)) << 4, vwB = (lane >> 3) * 1024 + vwc, vwA = (lane >> 3) * 3072 + vwc;
    const unsigned xrow = (bi * 64 + 8 * g) * 1024;
    auto issue = [&](int s) {
      const unsigned m = (1 + (s - 1) % 3) * 32768 + g * 1024;
      if (s <= 4) {
        const unsigned kc = ((s + bi + bj) & 3) * 128, wrow = (kc + 8 * g) * 1024 + bj * 128;
        dma(m, vx, rBx, xrow + kc * 2); dma(m + 4096, vx, rBx, xrow + 32768 + kc * 2);
        dma(m + 8192, vwB, rBq, wrow); dma(m + 12288, vwB, rBq, wrow + 32768);
        dma(m + 16384, vx, rBx, xrow + kc * 2 + 128); dma(m + 20480, vx, rBx, xrow + 32768 + kc * 2 + 128);
        dma(m + 24576, vwB, rBq, wrow + 65536); dma(m + 28672, vwB, rBq, wrow + 98304);
      } else if (s <= 12) {
        const unsigned kc = ((s + bi + 2 * bj) & 7) * 64, wrow = (kc + 8 * g) * 3072 + bj * 128;
        dma(m, vx, rAx, xrow + kc * 2); dma(m + 4096, vx, rAx, xrow + 32768 + kc * 2);
#pragma unroll
        for (int j = 0; j < 3; j++) { dma(m + 8192 + j * 8192, vwA, rAw, wrow + j * 1024); dma(m + 12288 + j * 8192, vwA, rAw, wrow + j * 1024 + 98304); }
      } else {
#pragma unroll
        for (int j = 0; j < 8; j++) dma_sc1(m + j * 4096, lane * 16, ws, WS_KV + m + j * 4096);
      }
    };
    issue(1); issue(2);
    asm volatile("s_barrier" ::: "memory");
    const unsigned xa0 = (32 * rh + (lane & 31)) * 128 + (((lane >> 5) ^ ((lane >> 1) & 7)) << 4);
    const unsigned wa0 = (8 * (lane >> 5) + q4) * 128 + (((4 * ch + 2 * (g4 & 1) + (p4 >> 1)) ^ ((q4 >> 1) << 2)) << 4) + 8 * (p4 & 1);
    f32x16 aq = {}, ak = {}, av = {};
    u32x2 ev;
    unsigned gb = 0;
    for (int s = 1; s <= 12; s++) {
      asm volatile("s_waitcnt vmcnt(8) lgkmcnt(0)" ::: "memory");
      cnt_add(C_GB, 1); gb += 4; cnt_wait(L_CNT + 4 * C_GB, gb);
      if (s == 1) ST(1);
      if (s == 10) dma_sc1(L_FLG + g * 1024, lane * 16, ws, WS_FKV + g * 1024);
      if (s == 11) {
        cnt_wait(L_CNT + 4 * C_KVP, 8);
        ev = *(const LAS u32x2*)(L_CNT + 4 * C_E);
        const unsigned fa = L_FLG + (lane >> 1) * 128 + (lane & 1) * 32;
        const u32x4 f0 = ldsr128(fa), f1 = ldsr128(fa + 16);
        const bool ok = f0[0] == ev[0] && f0[2] == ev[0] && f1[0] == ev[0] && f1[2] == ev[0] && f0[1] == ev[1] && f0[3] == ev[1] && f1[1] == ev[1] && f1[3] == ev[1];
        if (!__all(ok))
          for (int i = 0; i < 4; i++) flag_wait(ws, WS_FKV + (i * 8 + (lane >> 3)) * 128 + (lane & 7) * 8, ev);
      }
      issue(s + 2);
      const unsigned sl = (1 + (s - 1) % 3) * 32768, xa = xa0 + sl, wa = wa0 + sl;
      if (s <= 4) {
#pragma unroll
        for (int hf = 0; hf < 2; hf++)
#pragma unroll
          for (int ks = 0; ks < 4; ks++)
            aq = mfma32(ldstr2(wa + hf * 16384 + 8192 + ks * 2048, 512), ldsr128((xa ^ (ks * 32)) + hf * 16384), aq);
        if (s == 4) {
          aq *= QSCALE;
          ldsw128(L_Q + g * 2048 + lane * 16, pack8(aq, 0)); ldsw128(L_Q + g * 2048 + 1024 + lane * 16, pack8(aq, 8));
          cnt_add(C_Q + g, 1);
          aq = f32x16{};
          ST(2);
        }
      } else {
#pragma unroll
        for (int ks = 0; ks < 4; ks++) {
          const u32x4 xf = ldsr128(xa ^ (ks * 32));
          aq = mfma32(ldstr2(wa + 8192 + ks * 2048, 512), xf, aq);
          ak = mfma32(xf, ldstr2(wa + 16384 + ks * 2048, 512), ak);
          av = mfma32(xf, ldstr2(wa + 24576 + ks * 2048, 512), av);
        }
      }
    }
    asm volatile("s_waitcnt vmcnt(8) lgkmcnt(0)" ::: "memory");
    cnt_add(C_CH + 1, 1);
    cnt_add(C_GB, 1); gb += 4; cnt_wait(L_CNT + 4 * C_GB, gb);
    issue(15);
    ST(3);
    u32x4 pqa, pqb;
    {
#pragma unroll
      for (int i = 0; i < 16; i++) { aq[i] = phi(aq[i]); ak[i] = phi(ak[i]); }
      pqa = pack8(aq, 0); pqb = pack8(aq, 8);
      const u32x4 k0 = pack8(ak, 0), k1 = pack8(ak, 8), ones = {0x3f803f80u, 0x3f803f80u, 0x3f803f80u, 0x3f803f80u};
      f32x16 kvt = mfma32(k0, pack8(av, 0), f32x16{}); kvt = mfma32(k1, pack8(av, 8), kvt);
      f32x16 zt = mfma32(k0, ones, f32x16{}); zt = mfma32(k1, ones, zt);
      const bool hi = lane & 16;
#pragma unroll
      for (int r4 = 0; r4 < 4; r4++) {
        f32x4 v;
#pragma unroll
        for (int i = 0; i < 4; i++) v[i] = (hi != (r4 >= 2)) ? zt[4 * r4 + i] : kvt[4 * r4 + i];
        __builtin_amdgcn_raw_buffer_store_b128(__builtin_bit_cast(u32x4, v), ws, lane * 16 + r4 * 1024, WS_PART + (jc * 64 + rho) * 4096, SC1);
      }
    }
    const unsigned vo = ((lane >> 4) & 1) * 16384 + (lane >> 5) * 256 + (lane & 15) * 16, so = ((bi * 4 + rh * 2) * 16 + jc) * 1024;
    {
      const u32x4 sent = {~0u, ~0u, ~0u, ~0u};
      __builtin_amdgcn_raw_buffer_store_b128(sent, ws, vo, WS_OB + so, SC1);
      __builtin_amdgcn_raw_buffer_store_b128(sent, ws, vo + 512, WS_OB + so, SC1);
    }
    flag_set(ws, WS_FPART + (jc * 64 + rho) * 8, ev, lane);
    cnt_add(C_CH + 2, 1); cnt_add(C_CH + 3, 1);
    if (blockIdx.x == 0 && g == 0 && lane == 0) __builtin_amdgcn_raw_buffer_store_b64(ev, ws, WS_EPOCH, 0, SC1);
    ST(4);
    flag_wait(ws, WS_FPART + (jc * 64 + lane) * 8, ev);
    {
      const unsigned vp = (lane >> 2) * 4096 + p4 * 16, sp = WS_PART + jc * 262144 + (rho >> 4) * 1024 + (rho & 15) * 64;
      f32x4 s = __builtin_bit_cast(f32x4, __builtin_amdgcn_raw_buffer_load_b128(ws, vp, sp, SC1));
#pragma unroll
      for (int n = 1; n < 4; n++) s += __builtin_bit_cast(f32x4, __builtin_amdgcn_raw_buffer_load_b128(ws, vp, sp + n * 65536, SC1));
#pragma unroll
      for (int d = 4; d < 64; d *= 2)
#pragma unroll
        for (int i = 0; i < 4; i++) s[i] += __shfl_xor(s[i], d);
      if (lane < 4) __builtin_amdgcn_raw_buffer_store_b64(u32x2{cvtpk(s[0], s[1]), cvtpk(s[2], s[3])}, ws, lane * 16,
                      WS_KVF + jc * 2048 + (rho >> 5) * 1024 + (rho & 15) * 64 + ((rho >> 4) & 1) * 8, SC1);
      flag_set(ws, WS_FRED + (jc * 64 + rho) * 8, ev, lane);
    }
    ST(5);
    flag_wait(ws, WS_FRED + (jc * 64 + lane) * 8, ev);
    {
      const u32x4 fa = __builtin_amdgcn_raw_buffer_load_b128(ws, lane * 16, WS_KVF + jc * 2048, SC1);
      const u32x4 fb = __builtin_amdgcn_raw_buffer_load_b128(ws, lane * 16 + 1024, WS_KVF + jc * 2048, SC1);
      f32x16 da = mfma32(fa, pqa, f32x16{}), db = mfma32(fb, pqb, f32x16{});
      da *= __builtin_amdgcn_rcpf(da[8] + 1e-6f); db *= __builtin_amdgcn_rcpf(db[0] + 1e-6f);
      __builtin_amdgcn_raw_buffer_store_b128(nosent(pack8(da, 0)), ws, vo, WS_OA + so, SC1);
      __builtin_amdgcn_raw_buffer_store_b128(nosent(pack8(db, 8)), ws, vo + 512, WS_OA + so, SC1);
      flag_set(ws, WS_FOA + (rho * 16 + jc) * 8, ev, lane);
    }
    ST(6);
    cnt_wait(L_CNT + 4 * (C_PR + 1), 512);
    {
      const rsrc_t rW = __builtin_amdgcn_make_buffer_rsrc((void*)Awo, 0, 512 * 512 * 2, 0x20000);
      const unsigned vw = (lane >> 3) * 1024 + (((lane & 7) ^ (g4 << 1)) << 4);
#pragma unroll
      for (int j = 0; j < 16; j++) dma(g * 16384 + j * 1024, vw, rW, (g * 128 + j * 8) * 1024 + bj * 128);
    }
    flag_wait(ws, WS_FOA + ((bi * 2 + (g >> 1)) * 16 + (lane & 15)) * 8, ev);
    WAIT_VM(0);
    cnt_add(C_GB, 1); gb += 4; cnt_wait(L_CNT + 4 * C_GB, gb);
    ST(7);
    rq = g; u0 = 0; u1 = 2;
  }
  f32x16 acc = {};
  for (int u = u0; u < u1; u++) {
    const unsigned wb = u * 32768 + (16 * (g4 >> 1) + 4 * (g4 & 1) + q4) * 128 + ((2 * (g4 & 1) + (q4 >> 1)) << 5) + 8 * p4;
    const unsigned so = (isT ? WS_OB : WS_OA) + ((bi * 4 + rq) * 16 + 8 * u) * 1024;
    unsigned t0, f, sp;
    asm volatile(
"ds_read_b64_tr_b16 a[0:1], %[wa0] offset:0\n\t"
"ds_read_b64_tr_b16 a[2:3], %[wa0] offset:1024\n\t"
"ds_read_b64_tr_b16 a[4:5], %[wa1] offset:0\n\t"
"ds_read_b64_tr_b16 a[6:7], %[wa1] offset:1024\n\t"
"ds_read_b64_tr_b16 a[8:9], %[wa2] offset:0\n\t"
"ds_read_b64_tr_b16 a[10:11], %[wa2] offset:1024\n\t"
"ds_read_b64_tr_b16 a[12:13], %[wa3] offset:0\n\t"
"ds_read_b64_tr_b16 a[14:15], %[wa3] offset:1024\n\t"
"ds_read_b64_tr_b16 a[16:17], %[wa0] offset:4096\n\t"
"ds_read_b64_tr_b16 a[18:19], %[wa0] offset:5120\n\t"
"ds_read_b64_tr_b16 a[20:21], %[wa1] offset:4096\n\t"
"ds_read_b64_tr_b16 a[22:23], %[wa1] offset:5120\n\t"
"ds_read_b64_tr_b16 a[24:25], %[wa2] offset:4096\n\t"
"ds_read_b64_tr_b16 a[26:27], %[wa2] offset:5120\n\t"
"ds_read_b64_tr_b16 a[28:29], %[wa3] offset:4096\n\t"
"ds_read_b64_tr_b16 a[30:31], %[wa3] offset:5120\n\t"
"ds_read_b64_tr_b16 a[32:33], %[wa0] offset:8192\n\t"
"ds_read_b64_tr_b16 a[34:35], %[wa0] offset:9216\n\t"
"ds_read_b64_tr_b16 a[36:37], %[wa1] offset:8192\n\t"
"ds_read_b64_tr_b16 a[38:39], %[wa1] offset:9216\n\t"
"ds_read_b64_tr_b16 a[40:41], %[wa2] offset:8192\n\t"
"ds_read_b64_tr_b16 a[42:43], %[wa2] offset:9216\n\t"
"ds_read_b64_tr_b16 a[44:45], %[wa3] offset:8192\n\t"
"ds_read_b64_tr_b16 a[46:47], %[wa3] offset:9216\n\t"
"ds_read_b64_tr_b16 a[48:49], %[wa0] offset:12288\n\t"
"ds_read_b64_tr_b16 a[50:51], %[wa0] offset:13312\n\t"
"ds_read_b64_tr_b16 a[52:53], %[wa1] offset:12288\n\t"
"ds_read_b64_tr_b16 a[54:55], %[wa1] offset:13312\n\t"
"ds_read_b64_tr_b16 a[56:57], %[wa2] offset:12288\n\t"
"ds_read_b64_tr_b16 a[58:59], %[wa2] offset:13312\n\t"
"ds_read_b64_tr_b16 a[60:61], %[wa3] offset:12288\n\t"
"ds_read_b64_tr_b16 a[62:63], %[wa3] offset:13312\n\t"
"s_mov_b32 %[sp], 0x100000\n\t"
"s_mov_b32 %[f], 0\n\t"
"10:\n\t"
"buffer_load_dwordx4 v[16:19], %[oa], %[srd], %[so0] offen offset:0 sc1\n\t"
"11:\n\t"
"buffer_load_dwordx4 v[20:23], %[oa], %[srd], %[so0] offen offset:1024 sc1\n\t"
"12:\n\t"
"buffer_load_dwordx4 v[24:27], %[oa], %[srd], %[so0] offen offset:2048 sc1\n\t"
"13:\n\t"
"buffer_load_dwordx4 v[28:31], %[oa], %[srd], %[so0] offen offset:3072 sc1\n\t"
"14:\n\t"
"buffer_load_dwordx4 v[32:35], %[oa], %[srd], %[so1] offen offset:0 sc1\n\t"
"15:\n\t"
"buffer_load_dwordx4 v[36:39], %[oa], %[srd], %[so1] offen offset:1024 sc1\n\t"
"16:\n\t"
"buffer_load_dwordx4 v[40:43], %[oa], %[srd], %[so1] offen offset:2048 sc1\n\t"
"17:\n\t"
"buffer_load_dwordx4 v[44:47], %[oa], %[srd], %[so1] offen offset:3072 sc1\n\t"
"s_cmp_eq_u32 %[f], 1\n\t"
"s_cbranch_scc1 21f\n\t"
"s_cmp_eq_u32 %[f], 2\n\t"
"s_cbranch_scc1 22f\n\t"
"s_cmp_eq_u32 %[f], 3\n\t"
"s_cbranch_scc1 23f\n\t"
"s_cmp_eq_u32 %[f], 4\n\t"
"s_cbranch_scc1 24f\n\t"
"s_cmp_eq_u32 %[f], 5\n\t"
"s_cbranch_scc1 25f\n\t"
"s_cmp_eq_u32 %[f], 6\n\t"
"s_cbranch_scc1 26f\n\t"
"s_cmp_eq_u32 %[f], 7\n\t"
"s_cbranch_scc1 27f\n\t"
"20:\n\t"
"s_waitcnt vmcnt(7)\n\t"
"v_max3_u32 %[t0], v16, v17, v18\n\t"
"v_max_u32 %[t0], %[t0], v19\n\t"
"v_cmp_eq_u32 vcc, -1, %[t0]\n\t"
"s_cbranch_vccnz 30f\n\t"
"40:\n\t"
"s_waitcnt lgkmcnt(0)\n\t"
"v_mfma_f32_16x16x32_bf16 v[48:51], a[0:3], v[16:19], v[48:51]\n\t"
"v_mfma_f32_16x16x32_bf16 v[52:55], a[4:7], v[16:19], v[52:55]\n\t"
"v_mfma_f32_16x16x32_bf16 v[56:59], a[8:11], v[16:19], v[56:59]\n\t"
"v_mfma_f32_16x16x32_bf16 v[60:63], a[12:15], v[16:19], v[60:63]\n\t"
"ds_read_b64_tr_b16 a[0:1], %[wa0] offset:16384\n\t"
"ds_read_b64_tr_b16 a[2:3], %[wa0] offset:17408\n\t"
"ds_read_b64_tr_b16 a[4:5], %[wa1] offset:16384\n\t"
"ds_read_b64_tr_b16 a[6:7], %[wa1] offset:17408\n\t"
"ds_read_b64_tr_b16 a[8:9], %[wa2] offset:16384\n\t"
"ds_read_b64_tr_b16 a[10:11], %[wa2] offset:17408\n\t"
"ds_read_b64_tr_b16 a[12:13], %[wa3] offset:16384\n\t"
"ds_read_b64_tr_b16 a[14:15], %[wa3] offset:17408\n\t"
"21:\n\t"
"s_waitcnt vmcnt(6)\n\t"
"v_max3_u32 %[t0], v20, v21, v22\n\t"
"v_max_u32 %[t0], %[t0], v23\n\t"
"v_cmp_eq_u32 vcc, -1, %[t0]\n\t"
"s_cbranch_vccnz 31f\n\t"
"41:\n\t"
"v_mfma_f32_16x16x32_bf16 v[48:51], a[16:19], v[20:23], v[48:51]\n\t"
"v_mfma_f32_16x16x32_bf16 v[52:55], a[20:23], v[20:23], v[52:55]\n\t"
"v_mfma_f32_16x16x32_bf16 v[56:59], a[24:27], v[20:23], v[56:59]\n\t"
"v_mfma_f32_16x16x32_bf16 v[60:63], a[28:31], v[20:23], v[60:63]\n\t"
"ds_read_b64_tr_b16 a[16:17], %[wa0] offset:20480\n\t"
"ds_read_b64_tr_b16 a[18:19], %[wa0] offset:21504\n\t"
"ds_read_b64_tr_b16 a[20:21], %[wa1] offset:20480\n\t"
"ds_read_b64_tr_b16 a[22:23], %[wa1] offset:21504\n\t"
"ds_read_b64_tr_b16 a[24:25], %[wa2] offset:20480\n\t"
"ds_read_b64_tr_b16 a[26:27], %[wa2] offset:21504\n\t"
"ds_read_b64_tr_b16 a[28:29], %[wa3] offset:20480\n\t"
"ds_read_b64_tr_b16 a[30:31], %[wa3] offset:21504\n\t"
"22:\n\t"
"s_waitcnt vmcnt(5)\n\t"
"v_max3_u32 %[t0], v24, v25, v26\n\t"
"v_max_u32 %[t0], %[t0], v27\n\t"
"v_cmp_eq_u32 vcc, -1, %[t0]\n\t"
"s_cbranch_vccnz 32f\n\t"
"42:\n\t"
"v_mfma_f32_16x16x32_bf16 v[48:51], a[32:35], v[24:27], v[48:51]\n\t"
"v_mfma_f32_16x16x32_bf16 v[52:55], a[36:39], v[24:27], v[52:55]\n\t"
"v_mfma_f32_16x16x32_bf16 v[56:59], a[40:43], v[24:27], v[56:59]\n\t"
"v_mfma_f32_16x16x32_bf16 v[60:63], a[44:47], v[24:27], v[60:63]\n\t"
"ds_read_b64_tr_b16 a[32:33], %[wa0] offset:24576\n\t"
"ds_read_b64_tr_b16 a[34:35], %[wa0] offset:25600\n\t"
"ds_read_b64_tr_b16 a[36:37], %[wa1] offset:24576\n\t"
"ds_read_b64_tr_b16 a[38:39], %[wa1] offset:25600\n\t"
"ds_read_b64_tr_b16 a[40:41], %[wa2] offset:24576\n\t"
"ds_read_b64_tr_b16 a[42:43], %[wa2] offset:25600\n\t"
"ds_read_b64_tr_b16 a[44:45], %[wa3] offset:24576\n\t"
"ds_read_b64_tr_b16 a[46:47], %[wa3] offset:25600\n\t"
"23:\n\t"
"s_waitcnt vmcnt(4)\n\t"
"v_max3_u32 %[t0], v28, v29, v30\n\t"
"v_max_u32 %[t0], %[t0], v31\n\t"
"v_cmp_eq_u32 vcc, -1, %[t0]\n\t"
"s_cbranch_vccnz 33f\n\t"
"43:\n\t"
"v_mfma_f32_16x16x32_bf16 v[48:51], a[48:51], v[28:31], v[48:51]\n\t"
"v_mfma_f32_16x16x32_bf16 v[52:55], a[52:55], v[28:31], v[52:55]\n\t"
"v_mfma_f32_16x16x32_bf16 v[56:59], a[56:59], v[28:31], v[56:59]\n\t"
"v_mfma_f32_16x16x32_bf16 v[60:63], a[60:63], v[28:31], v[60:63]\n\t"
"ds_read_b64_tr_b16 a[48:49], %[wa0] offset:28672\n\t"
"ds_read_b64_tr_b16 a[50:51], %[wa0] offset:29696\n\t"
"ds_read_b64_tr_b16 a[52:53], %[wa1] offset:28672\n\t"
"ds_read_b64_tr_b16 a[54:55], %[wa1] offset:29696\n\t"
"ds_read_b64_tr_b16 a[56:57], %[wa2] offset:28672\n\t"
"ds_read_b64_tr_b16 a[58:59], %[wa2] offset:29696\n\t"
"ds_read_b64_tr_b16 a[60:61], %[wa3] offset:28672\n\t"
"ds_read_b64_tr_b16 a[62:63], %[wa3] offset:29696\n\t"
"24:\n\t"
"s_waitcnt vmcnt(3)\n\t"
"v_max3_u32 %[t0], v32, v33, v34\n\t"
"v_max_u32 %[t0], %[t0], v35\n\t"
"v_cmp_eq_u32 vcc, -1, %[t0]\n\t"
"s_cbranch_vccnz 34f\n\t"
"44:\n\t"
"s_waitcnt lgkmcnt(15)\n\t"
"v_mfma_f32_16x16x32_bf16 v[48:51], a[0:3], v[32:35], v[48:51]\n\t"
"v_mfma_f32_16x16x32_bf16 v[52:55], a[4:7], v[32:35], v[52:55]\n\t"
"v_mfma_f32_16x16x32_bf16 v[56:59], a[8:11], v[32:35], v[56:59]\n\t"
"v_mfma_f32_16x16x32_bf16 v[60:63], a[12:15], v[32:35], v[60:63]\n\t"
"25:\n\t"
"s_waitcnt vmcnt(2)\n\t"
"v_max3_u32 %[t0], v36, v37, v38\n\t"
"v_max_u32 %[t0], %[t0], v39\n\t"
"v_cmp_eq_u32 vcc, -1, %[t0]\n\t"
"s_cbranch_vccnz 35f\n\t"
"45:\n\t"
"v_mfma_f32_16x16x32_bf16 v[48:51], a[16:19], v[36:39], v[48:51]\n\t"
"v_mfma_f32_16x16x32_bf16 v[52:55], a[20:23], v[36:39], v[52:55]\n\t"
"v_mfma_f32_16x16x32_bf16 v[56:59], a[24:27], v[36:39], v[56:59]\n\t"
"v_mfma_f32_16x16x32_bf16 v[60:63], a[28:31], v[36:39], v[60:63]\n\t"
"26:\n\t"
"s_waitcnt vmcnt(1)\n\t"
"v_max3_u32 %[t0], v40, v41, v42\n\t"
"v_max_u32 %[t0], %[t0], v43\n\t"
"v_cmp_eq_u32 vcc, -1, %[t0]\n\t"
"s_cbranch_vccnz 36f\n\t"
"46:\n\t"
"s_waitcnt lgkmcnt(8)\n\t"
"v_mfma_f32_16x16x32_bf16 v[48:51], a[32:35], v[40:43], v[48:51]\n\t"
"v_mfma_f32_16x16x32_bf16 v[52:55], a[36:39], v[40:43], v[52:55]\n\t"
"v_mfma_f32_16x16x32_bf16 v[56:59], a[40:43], v[40:43], v[56:59]\n\t"
"v_mfma_f32_16x16x32_bf16 v[60:63], a[44:47], v[40:43], v[60:63]\n\t"
"27:\n\t"
"s_waitcnt vmcnt(0)\n\t"
"v_max3_u32 %[t0], v44, v45, v46\n\t"
"v_max_u32 %[t0], %[t0], v47\n\t"
"v_cmp_eq_u32 vcc, -1, %[t0]\n\t"
"s_cbranch_vccnz 37f\n\t"
"47:\n\t"
"s_waitcnt lgkmcnt(0)\n\t"
"v_mfma_f32_16x16x32_bf16 v[48:51], a[48:51], v[44:47], v[48:51]\n\t"
"v_mfma_f32_16x16x32_bf16 v[52:55], a[52:55], v[44:47], v[52:55]\n\t"
"v_mfma_f32_16x16x32_bf16 v[56:59], a[56:59], v[44:47], v[56:59]\n\t"
"v_mfma_f32_16x16x32_bf16 v[60:63], a[60:63], v[44:47], v[60:63]\n\t"
"s_branch 50f\n\t"
"30:\n\t"
"s_sub_u32 %[sp], %[sp], 1\n\t"
"s_cmp_le_i32 %[sp], 0\n\t"
"s_cbranch_scc1 40b\n\t"
"s_mov_b32 %[f], 0\n\t"
"s_branch 10b\n\t"
"31:\n\t"
"s_sub_u32 %[sp], %[sp], 1\n\t"
"s_cmp_le_i32 %[sp], 0\n\t"
"s_cbranch_scc1 41b\n\t"
"s_mov_b32 %[f], 1\n\t"
"s_branch 11b\n\t"
"32:\n\t"
"s_sub_u32 %[sp], %[sp], 1\n\t"
"s_cmp_le_i32 %[sp], 0\n\t"
"s_cbranch_scc1 42b\n\t"
"s_mov_b32 %[f], 2\n\t"
"s_branch 12b\n\t"
"33:\n\t"
"s_sub_u32 %[sp], %[sp], 1\n\t"
"s_cmp_le_i32 %[sp], 0\n\t"
"s_cbranch_scc1 43b\n\t"
"s_mov_b32 %[f], 3\n\t"
"s_branch 13b\n\t"
"34:\n\t"
"s_sub_u32 %[sp], %[sp], 1\n\t"
"s_cmp_le_i32 %[sp], 0\n\t"
"s_cbranch_scc1 44b\n\t"
"s_mov_b32 %[f], 4\n\t"
"s_branch 14b\n\t"
"35:\n\t"
"s_sub_u32 %[sp], %[sp], 1\n\t"
"s_cmp_le_i32 %[sp], 0\n\t"
"s_cbranch_scc1 45b\n\t"
"s_mov_b32 %[f], 5\n\t"
"s_branch 15b\n\t"
"36:\n\t"
"s_sub_u32 %[sp], %[sp], 1\n\t"
"s_cmp_le_i32 %[sp], 0\n\t"
"s_cbranch_scc1 46b\n\t"
"s_mov_b32 %[f], 6\n\t"
"s_branch 16b\n\t"
"37:\n\t"
"s_sub_u32 %[sp], %[sp], 1\n\t"
"s_cmp_le_i32 %[sp], 0\n\t"
"s_cbranch_scc1 47b\n\t"
"s_mov_b32 %[f], 7\n\t"
"s_branch 17b\n\t"
"50:\n\t"
"s_nop 7\n\t"
      : "+{v[48:63]}"(acc), [t0] "=&v"(t0), [f] "=&s"(f), [sp] "=&s"(sp)
      : [wa0] "v"(wb), [wa1] "v"(wb ^ 32), [wa2] "v"(wb ^ 64), [wa3] "v"(wb ^ 96), [oa] "v"(lane * 16), [srd] "s"(ws), [so0] "s"(so), [so1] "s"(so + 4096)
      : "v16","v17","v18","v19","v20","v21","v22","v23","v24","v25","v26","v27","v28","v29","v30","v31",
        "v32","v33","v34","v35","v36","v37","v38","v39","v40","v41","v42","v43","v44","v45","v46","v47",
        "a0","a1","a2","a3","a4","a5","a6","a7","a8","a9","a10","a11","a12","a13","a14","a15",
        "a16","a17","a18","a19","a20","a21","a22","a23","a24","a25","a26","a27","a28","a29","a30","a31",
        "a32","a33","a34","a35","a36","a37","a38","a39","a40","a41","a42","a43","a44","a45","a46","a47",
        "a48","a49","a50","a51","a52","a53","a54","a55","a56","a57","a58","a59","a60","a61","a62","a63","vcc","scc","memory");
  }
  ST(9);
  u16* orow = out + (size_t)(bi * 64 + rq * 16 + (lane & 15)) * 1024 + (isT ? 512 : 0) + bj * 64 + 16 * (g4 & 1) + 8 * (g4 >> 1);
  auto store_pair = [&](const float* a, int col) {
    auto s0 = __builtin_amdgcn_permlane16_swap(cvtpk(a[0], a[1]), cvtpk(a[4], a[5]), false, false);
    auto s1 = __builtin_amdgcn_permlane16_swap(cvtpk(a[2], a[3]), cvtpk(a[6], a[7]), false, false);
    *(u32x4*)(orow + col) = u32x4{s0[0], s1[0], s0[1], s1[1]};
  };
  if (isT) {
    const unsigned ex = L_EX + 18432 - 2048 * 0;
    (void)ex;
    const unsigned mine = 65536 + wv * 2048 + lane * 16, other = 65536 + (wv ^ 4) * 2048 + lane * 16;
    f32x4 s0, s1; float a[8];
#pragma unroll
    for (int i = 0; i < 4; i++) { s0[i] = u0 ? acc[i] : acc[8 + i]; s1[i] = u0 ? acc[4 + i] : acc[12 + i]; }
    cnt_wait(L_CNT + 4 * (C_PR + 2), 512);
    *(LAS f32x4*)mine = s0; *(LAS f32x4*)(mine + 1024) = s1;
    cnt_add(C_X2 + wv, 1);
    cnt_wait(L_CNT + 4 * (C_X2 + (wv ^ 4)), 1);
    s0 = *(const LAS f32x4*)other; s1 = *(const LAS f32x4*)(other + 1024);
#pragma unroll
    for (int i = 0; i < 4; i++) { a[i] = (u0 ? acc[8 + i] : acc[i]) + s0[i]; a[4 + i] = (u0 ? acc[12 + i] : acc[4 + i]) + s1[i]; }
    store_pair(a, u0 * 32);
  } else {
    float a[8], b[8];
#pragma unroll
    for (int i = 0; i < 8; i++) { a[i] = acc[i]; b[i] = acc[8 + i]; }
    store_pair(a, 0); store_pair(b, 32);
    const int g = wv - 8;
    cnt_add(C_GB, 1); cnt_wait(L_CNT + 4 * C_GB, 60);
    const rsrc_t rW = __builtin_amdgcn_make_buffer_rsrc((void*)Bwo, 0, 512 * 512 * 2, 0x20000);
    const unsigned vw = (lane >> 3) * 1024 + (((lane & 7) ^ (g4 << 1)) << 4);
#pragma unroll
    for (int j = 0; j < 16; j++) dma(g * 16384 + j * 1024, vw, rW, (g * 128 + j * 8) * 1024 + bj * 128);
    WAIT_VM(0);
    cnt_add(C_WB, 1);
  }
  ST(10);
}

extern "C" void kernel_launch(void* const* d_in, const int* in_sizes, int n_in, void* d_out, int out_size, void* d_ws, size_t ws_size,
                              hipStream_t stream) {
  (void)hipFuncSetAttribute((const void*)fused, hipFuncAttributeMaxDynamicSharedMemorySize, LDS_BYTES);
  fused<<<256, 768, LDS_BYTES, stream>>>((const u16*)d_in[0], (const u16*)d_in[1], (const u16*)d_in[2], (const u16*)d_in[3],
                                         (const u16*)d_in[4], (const u16*)d_in[5], (const u16*)d_in[6], (const u16*)d_in[7],
                                         (u16*)d_out, d_ws);
}
```

```cpp
#include <hip/hip_runtime.h>
#include <stdint.h>
typedef unsigned short u16;
typedef unsigned long long u64;

__global__ void __launch_bounds__(768, 3)
fused(const void* Ax, const void* Awqkv, const void* Awo, const void* Bx, const void* Bwq, const void* Bwk, const void* Bwv, const void* Bwo,
      void* out, void* ws) {
  unsigned tid = threadIdx.x;
  asm volatile(
    "s_load_dwordx16 s[16:31], s[0:1], 0x0\n\t"
    "s_load_dwordx4 s[32:35], s[0:1], 0x40\n\t"
    "v_readfirstlane_b32 s3, v0\n\t"
    "v_and_b32 v0, 63, v0\n\t"
    "v_lshlrev_b32 v1, 4, v0\n\t"
    "s_lshl_b32 s6, s2, 2\n\t"
    "s_and_b32 s77, s6, 28\n\t"
    "s_lshr_b32 s7, s2, 6\n\t"
    "s_add_u32 s77, s77, s7\n\t"
    "s_bfe_u32 s78, s2, 0x30003\n\t"
    "s_lshr_b32 s3, s3, 6\n\t"
    "v_lshrrev_b32 v10, 3, v0\n\t"
    "v_lshrrev_b32 v11, 4, v0\n\t"
    "v_and_b32 v14, 7, v0\n\t"
    "s_cmp_lt_u32 s3, 8\n\t"
    "s_cbranch_scc1 .LXT1_%=\n\t"
    "s_setprio 3\n\t"
    "s_sub_u32 s58, s3, 8\n\t"
    "s_mov_b32 s42, 0x200000\n\t"
    "s_mov_b32 s43, 0x20000\n\t"
    "s_mov_b32 s46, 0x4000\n\t"
    "s_mov_b32 s47, 0x20000\n\t"
    "v_xor_b32 v2, v11, v14\n\t"
    "v_lshlrev_b32 v2, 4, v2\n\t"
    "v_lshl_add_u32 v2, v10, 10, v2\n\t"
    "v_xor_b32 v3, 64, v2\n\t"
    "s_add_u32 s10, s77, s78\n\t"
    "s_add_u32 s10, s10, s58\n\t"
    "s_and_b32 s10, s10, 3\n\t"
    "s_lshl_b32 s8, s77, 16\n\t"
    "s_lshr_b32 s7, s78, 1\n\t"
    "s_lshl_b32 s6, s7, 14\n\t"
    "s_add_u32 s8, s8, s6\n\t"
    "s_lshl_b32 s6, s10, 8\n\t"
    "s_add_u32 s8, s8, s6\n\t"
    "s_add_u32 s11, s8, 0x1c00\n\t"
    "s_lshl_b32 s6, s58, 2\n\t"
    "s_lshr_b32 s9, 0x1430, s6\n\t"
    "s_and_b32 s9, s9, 7\n\t"
    "s_lshl_b32 s9, s9, 15\n\t"
    "s_lshl_b32 s7, s7, 11\n\t"
    "s_add_u32 s9, s9, s7\n\t"
    "s_mov_b32 m0, s9\n\t"
    "s_lshl_b32 s10, s10, 12\n\t"
    "s_lshl_b32 s79, s58, 12\n\t"
    "s_add_u32 s79, s79, 81920\n\t"
    "s_waitcnt lgkmcnt(0)\n\t"
    "s_mov_b64 s[40:41], s[22:23]\n\t"
    "buffer_load_dwordx4 v2, s[40:43], s8 offen lds\n\t"
    "buffer_load_dwordx4 v3, s[40:43], s11 offen offset:1024 lds\n\t"
    "s_add_u32 m0, m0, 0x3f80\n\t"
    "s_mov_b64 s[36:37], s[34:35]\n\t"
    "s_mov_b32 s38, 10485760\n\t"
    "s_mov_b32 s39, 0x20000\n\t"
    "s_bitcmp1_b32 s78, 0\n\t"
    "s_cselect_b64 s[44:45], s[28:29], s[26:27]\n\t"
    "buffer_load_dwordx4 v2, s[40:43], s8 offen offset:128 lds\n\t"
    "buffer_load_dwordx4 v3, s[40:43], s11 offen offset:1152 lds\n\t"
    "buffer_load_dwordx2 v[4:5], off, s[36:39], 0 sc1\n\t"
    "s_mov_b32 m0, s79\n\t"
    "s_nop 0\n\t"
    "buffer_load_dwordx4 v1, s[44:47], s10 offen offset:0 lds\n\t"
    "buffer_load_dwordx4 v1, s[44:47], s10 offen offset:1024 lds\n\t"
    "buffer_load_dwordx4 v1, s[44:47], s10 offen offset:2048 lds\n\t"
    "buffer_load_dwordx4 v1, s[44:47], s10 offen offset:3072 lds\n\t"
    "s_branch .LXG2_%=\n\t"
    ".LXT1_%=:\n\t"
    "s_cmp_lg_u32 s3, 7\n\t"
    "s_cbranch_scc1 .LX8_%=\n\t"
    "v_and_b32 v5, 47, v0\n\t"
    "v_cmp_eq_u32 vcc, 4, v5\n\t"
    "v_lshlrev_b32 v5, 2, v0\n\t"
    "v_add_u32 v5, 65536, v5\n\t"
    "v_cndmask_b32 v6, 0, 4, vcc\n\t"
    "ds_write_b32 v5, v6\n\t"
    "s_getpc_b64 s[62:63]\n\t"
    ".LX9_%=:\n\t"
    "s_lshr_b32 s6, s2, 3\n\t"
    "s_lshl_b32 s6, s6, 10\n\t"
    "v_lshl_add_u32 v5, v0, 7, s6\n\t"
    "v_min_u32 v5, .LXend5_%=-.LX9_%=, v5\n\t"
    "s_mov_b32 m0, 65792\n\t"
    "s_mov_b64 exec, 0xff\n\t"
    "global_load_lds_dword v5, s[62:63]\n\t"
    "s_mov_b64 exec, -1\n\t"
    ".LX8_%=:\n\t"
    "s_and_b32 s6, s3, 1\n\t"
    "s_lshl_b32 s6, s6, 2\n\t"
    "v_add_u32 v2, s6, v11\n\t"
    "v_xor_b32 v2, v2, v14\n\t"
    "v_lshlrev_b32 v2, 4, v2\n\t"
    "v_lshl_add_u32 v2, v10, 10, v2\n\t"
    "v_lshlrev_b32 v4, 2, v11\n\t"
    "v_and_b32 v4, 4, v4\n\t"
    "v_xor_b32 v4, v4, v14\n\t"
    "v_lshlrev_b32 v4, 4, v4\n\t"
    "v_lshl_add_u32 v3, v10, 10, v4\n\t"
    "s_lshl_b32 s73, s77, 16\n\t"
    "s_lshl_b32 s6, s3, 13\n\t"
    "s_add_u32 s73, s73, s6\n\t"
    "s_lshl_b32 s7, s78, 7\n\t"
    "s_add_u32 s9, s6, s7\n\t"
    "s_add_u32 s11, s77, s78\n\t"
    "s_lshl_b32 s13, s3, 10\n\t"
    "s_waitcnt lgkmcnt(0)\n\t"
    "s_mov_b64 s[40:41], s[22:23]\n\t"
    "s_mov_b32 s42, 2097152\n\t"
    "s_mov_b32 s43, 0x20000\n\t"
    "s_mov_b64 s[44:45], s[24:25]\n\t"
    "s_mov_b32 s46, 524288\n\t"
    "s_mov_b32 s47, 0x20000\n\t"
    "s_barrier\n\t"
    "s_add_u32 m0, s13, 8192\n\t"
    "s_add_u32 s6, s11, 0\n\t"
    "s_and_b32 s6, s6, 3\n\t"
    "s_lshl_b32 s6, s6, 17\n\t"
    "s_add_u32 s6, s6, s9\n\t"
    "buffer_load_dwordx4 v3, s[44:47], s6 offen lds\n\t"
    "s_add_u32 m0, m0, 0x4000\n\t"
    "s_add_u32 s6, s6, 0x10000\n\t"
    "buffer_load_dwordx4 v3, s[44:47], s6 offen lds\n\t"
    "s_xor_b32 s6, s3, s78\n\t"
    "s_cmp_lt_u32 s6, 2\n\t"
    "s_cbranch_scc1 .LX10_%=\n\t"
    "s_add_u32 m0, s13, 0\n\t"
    "s_add_u32 s6, s11, 0\n\t"
    "s_and_b32 s6, s6, 3\n\t"
    "s_lshl_b32 s6, s6, 8\n\t"
    "s_add_u32 s6, s6, s73\n\t"
    "buffer_load_dwordx4 v2, s[40:43], s6 offen lds\n\t"
    "s_add_u32 m0, m0, 0x3f80\n\t"
    "s_nop 0\n\t"
    "buffer_load_dwordx4 v2, s[40:43], s6 offen offset:128 lds\n\t"
    ".LX10_%=:\n\t"
    "s_mov_b64 exec, 0\n\t"
    "s_cmp_eq_u32 s3, 0\n\t"
    "s_cbranch_scc1 .LXp172_%=\n\t"
    "s_cmp_eq_u32 s3, 1\n\t"
    "s_cbranch_scc1 .LXp173_%=\n\t"
    "s_cmp_eq_u32 s3, 2\n\t"
    "s_cbranch_scc1 .LXp174_%=\n\t"
    "s_cmp_eq_u32 s3, 3\n\t"
    "s_cbranch_scc1 .LXp175_%=\n\t"
    "s_cmp_eq_u32 s3, 4\n\t"
    "s_cbranch_scc1 .LXp176_%=\n\t"
    "s_cmp_eq_u32 s3, 5\n\t"
    "s_cbranch_scc1 .LXp177_%=\n\t"
    "s_cmp_eq_u32 s3, 6\n\t"
    "s_cbranch_scc1 .LXp178_%=\n\t"
    "s_branch .LXp179_%=\n\t"
    ".LXr204_%=:\n\t"
    "s_mov_b64 exec, -1\n\t"
    "s_lshl_b32 s6, s2, 2\n\t"
    "s_and_b32 s4, s6, 24\n\t"
    "s_lshr_b32 s7, s2, 5\n\t"
    "s_add_u32 s4, s4, s7\n\t"
    "s_and_b32 s5, s6, 4\n\t"
    "s_bfe_u32 s7, s2, 0x20003\n\t"
    "s_or_b32 s5, s5, s7\n\t"
    "v_mov_b32 v13, 1\n\t"
    "s_mov_b64 s[36:37], s[34:35]\n\t"
    "s_mov_b32 s38, 10485760\n\t"
    "s_mov_b32 s39, 0x20000\n\t"
    "s_mov_b64 s[48:49], s[16:17]\n\t"
    "s_mov_b32 s50, 2097152\n\t"
    "s_mov_b32 s51, 0x20000\n\t"
    "s_mov_b64 s[52:53], s[18:19]\n\t"
    "s_mov_b32 s54, 1572864\n\t"
    "s_mov_b32 s55, 0x20000\n\t"
    "s_and_b32 s58, s3, 3\n\t"
    "s_lshr_b32 s59, s3, 2\n\t"
    "s_mov_b32 s60, 0xffff\n\t"
    "s_mov_b32 s61, 0xffff\n\t"
    "v_mov_b32 v12, 0xdddd\n\t"
    "s_movk_i32 s12, 0xc00\n\t"
    "v_mad_u32_u24 v10, v10, s12, v4\n\t"
    "s_and_b32 s6, s4, 24\n\t"
    "s_add_u32 s6, s6, s3\n\t"
    "s_lshl_b32 s62, s6, 12\n\t"
    "s_add_u32 s8, s62, 65536\n\t"
    "s_lshl_b32 s10, s6, 10\n\t"
    "s_add_u32 s10, s10, 32768\n\t"
    "v_mov_b32 v4, 65536\n\t"
    "v_and_b32 v5, 27, v0\n\t"
    "v_cmp_eq_u32 vcc, 16, v5\n\t"
    "v_mov_b32 v7, 0x3f803f80\n\t"
    "s_nop 0\n\t"
    "v_cndmask_b32 v5, 0, v7, vcc\n\t"
    "v_accvgpr_write_b32 a0, 0\n\t"
    "v_accvgpr_write_b32 a1, 0\n\t"
    "v_accvgpr_write_b32 a2, 0\n\t"
    "v_accvgpr_write_b32 a3, 0\n\t"
    "v_accvgpr_write_b32 a4, 0\n\t"
    "v_accvgpr_write_b32 a5, 0\n\t"
    "v_accvgpr_write_b32 a6, 0\n\t"
    "v_accvgpr_write_b32 a7, 0\n\t"
    "v_accvgpr_write_b32 a8, 0\n\t"
    "v_accvgpr_write_b32 a9, 0\n\t"
    "v_accvgpr_write_b32 a10, 0\n\t"
    "v_accvgpr_write_b32 a11, 0\n\t"
    "v_accvgpr_write_b32 a12, 0\n\t"
    "v_accvgpr_write_b32 a13, 0\n\t"
    "v_accvgpr_write_b32 a14, 0\n\t"
    "v_accvgpr_write_b32 a15, 0\n\t"
    "v_accvgpr_write_b32 a16, 0\n\t"
    "v_accvgpr_write_b32 a17, 0\n\t"
    "v_accvgpr_write_b32 a18, 0\n\t"
    "v_accvgpr_write_b32 a19, 0\n\t"
    "v_accvgpr_write_b32 a20, 0\n\t"
    "v_accvgpr_write_b32 a21, 0\n\t"
    "v_accvgpr_write_b32 a22, 0\n\t"
    "v_accvgpr_write_b32 a23, 0\n\t"
    "v_accvgpr_write_b32 a24, 0\n\t"
    "v_accvgpr_write_b32 a25, 0\n\t"
    "v_accvgpr_write_b32 a26, 0\n\t"
    "v_accvgpr_write_b32 a27, 0\n\t"
    "v_accvgpr_write_b32 a28, 0\n\t"
    "v_accvgpr_write_b32 a29, 0\n\t"
    "v_accvgpr_write_b32 a30, 0\n\t"
    "v_accvgpr_write_b32 a31, 0\n\t"
    "v_accvgpr_write_b32 a48, v5\n\t"
    "v_accvgpr_write_b32 a49, v5\n\t"
    ".LXp172_%=:\n\t"
    "s_cbranch_execz .LXp180_%=\n\t"
    "v_accvgpr_write_b32 a50, v5\n\t"
    "v_accvgpr_write_b32 a51, v5\n\t"
    "v_accvgpr_write_b32 a52, v5\n\t"
    "v_accvgpr_write_b32 a53, v5\n\t"
    "v_accvgpr_write_b32 a54, v5\n\t"
    "v_accvgpr_write_b32 a55, v5\n\t"
    "v_accvgpr_write_b32 a56, v5\n\t"
    "v_accvgpr_write_b32 a57, v5\n\t"
    "v_accvgpr_write_b32 a58, v5\n\t"
    "v_accvgpr_write_b32 a59, v5\n\t"
    "v_accvgpr_write_b32 a60, v5\n\t"
    "v_accvgpr_write_b32 a61, v5\n\t"
    "v_accvgpr_write_b32 a62, v5\n\t"
    "v_accvgpr_write_b32 a63, v5\n\t"
    "s_xor_b32 s6, s3, s78\n\t"
    "s_cmp_lt_u32 s6, 2\n\t"
    "s_cbranch_scc1 .LX11_%=\n\t"
    "s_add_u32 m0, s13, 98304\n\t"
    "s_add_u32 s6, s11, 1\n\t"
    "s_and_b32 s6, s6, 3\n\t"
    "s_lshl_b32 s6, s6, 8\n\t"
    "s_add_u32 s6, s6, s73\n\t"
    "buffer_load_dwordx4 v2, s[40:43], s6 offen lds\n\t"
    "s_add_u32 m0, m0, 0x3f80\n\t"
    "s_nop 0\n\t"
    "buffer_load_dwordx4 v2, s[40:43], s6 offen offset:128 lds\n\t"
    ".LX11_%=:\n\t"
    "s_add_u32 m0, s13, 106496\n\t"
    "s_add_u32 s6, s11, 1\n\t"
    "s_and_b32 s6, s6, 3\n\t"
    "s_lshl_b32 s6, s6, 17\n\t"
    "s_add_u32 s6, s6, s9\n\t"
    "buffer_load_dwordx4 v3, s[44:47], s6 offen lds\n\t"
    "s_add_u32 m0, m0, 0x4000\n\t"
    "s_add_u32 s6, s6, 0x10000\n\t"
    "buffer_load_dwordx4 v3, s[44:47], s6 offen lds\n\t"
    "s_waitcnt vmcnt(4)\n\t"
    "s_xor_b32 s6, s3, s78\n\t"
    "s_cmp_gt_u32 s6, 1\n\t"
    "s_cbranch_scc1 .LX12_%=\n\t"
    "s_waitcnt vmcnt(2)\n\t"
    ".LX12_%=:\n\t"
    "s_barrier\n\t"
    "ds_read_b32 v14, v4 offset:136\n\t"
    "s_xor_b32 s6, s3, s78\n\t"
    "s_cmp_lt_u32 s6, 2\n\t"
    "s_cbranch_scc1 .LX13_%=\n\t"
    "s_add_u32 m0, s13, 131072\n\t"
    "s_add_u32 s6, s11, 2\n\t"
    "s_and_b32 s6, s6, 3\n\t"
    "s_lshl_b32 s6, s6, 8\n\t"
    "s_add_u32 s6, s6, s73\n\t"
    "buffer_load_dwordx4 v2, s[40:43], s6 offen lds\n\t"
    "s_add_u32 m0, m0, 0x3f80\n\t"
    "s_nop 0\n\t"
    "buffer_load_dwordx4 v2, s[40:43], s6 offen offset:128 lds\n\t"
    ".LX13_%=:\n\t"
    "s_add_u32 m0, s13, 139264\n\t"
    "s_add_u32 s6, s11, 2\n\t"
    "s_and_b32 s6, s6, 3\n\t"
    "s_lshl_b32 s6, s6, 17\n\t"
    "s_add_u32 s6, s6, s9\n\t"
    "buffer_load_dwordx4 v3, s[44:47], s6 offen lds\n\t"
    "s_add_u32 m0, m0, 0x4000\n\t"
    "s_add_u32 s6, s6, 0x10000\n\t"
    "buffer_load_dwordx4 v3, s[44:47], s6 offen lds\n\t"
    "s_waitcnt vmcnt(4)\n\t"
    "s_xor_b32 s6, s3, s78\n\t"
    "s_cmp_gt_u32 s6, 1\n\t"
    "s_cbranch_scc1 .LX14_%=\n\t"
    "s_waitcnt vmcnt(2)\n\t"
    ".LX14_%=:\n\t"
    "s_barrier\n\t"
    "s_xor_b32 s6, s3, s78\n\t"
    "s_cmp_lt_u32 s6, 2\n\t"
    "s_cbranch_scc1 .LX15_%=\n\t"
    "s_add_u32 m0, s13, 32768\n\t"
    "s_add_u32 s6, s11, 3\n\t"
    "s_and_b32 s6, s6, 3\n\t"
    "s_lshl_b32 s6, s6, 8\n\t"
    "s_add_u32 s6, s6, s73\n\t"
    "buffer_load_dwordx4 v2, s[40:43], s6 offen lds\n\t"
    "s_add_u32 m0, m0, 0x3f80\n\t"
    "s_nop 0\n\t"
    "buffer_load_dwordx4 v2, s[40:43], s6 offen offset:128 lds\n\t"
    ".LX15_%=:\n\t"
    "s_add_u32 m0, s13, 40960\n\t"
    "s_add_u32 s6, s11, 3\n\t"
    "s_and_b32 s6, s6, 3\n\t"
    "s_lshl_b32 s6, s6, 17\n\t"
    ".LXp173_%=:\n\t"
    "s_cbranch_execz .LXp181_%=\n\t"
    "s_add_u32 s6, s6, s9\n\t"
    "buffer_load_dwordx4 v3, s[44:47], s6 offen lds\n\t"
    "s_add_u32 m0, m0, 0x4000\n\t"
    "s_add_u32 s6, s6, 0x10000\n\t"
    "buffer_load_dwordx4 v3, s[44:47], s6 offen lds\n\t"
    "s_waitcnt vmcnt(4)\n\t"
    "s_xor_b32 s6, s3, s78\n\t"
    "s_cmp_gt_u32 s6, 1\n\t"
    "s_cbranch_scc1 .LX16_%=\n\t"
    "s_waitcnt vmcnt(2)\n\t"
    ".LX16_%=:\n\t"
    "s_barrier\n\t"
    "buffer_load_dwordx4 v[64:67], v1, s[36:39], s8 offen offset:0 sc1\n\t"
    "buffer_load_dwordx4 v[68:71], v1, s[36:39], s8 offen offset:1024 sc1\n\t"
    "buffer_load_dwordx4 v[72:75], v1, s[36:39], s8 offen offset:2048 sc1\n\t"
    "buffer_load_dwordx4 v[76:79], v1, s[36:39], s8 offen offset:3072 sc1\n\t"
    "buffer_load_dwordx4 v[80:83], v1, s[36:39], s10 offen sc1\n\t"
    "s_waitcnt vmcnt(5) lgkmcnt(0)\n\t"
    "s_barrier\n\t"
    "v_readfirstlane_b32 s56, v14\n\t"
    "s_lshl_b32 s6, s3, 8\n\t"
    "s_add_u32 s6, s6, 67584\n\t"
    "v_lshl_add_u32 v9, v0, 2, s6\n\t"
    "s_lshl_b32 s6, s3, 10\n\t"
    "s_add_u32 s6, s6, 73728\n\t"
    "v_add_u32 v7, s6, v1\n\t"
    "v_xor_b32 v5, 0x1000, v7\n\t"
    "s_lshl_b32 s9, s3, 12\n\t"
    "v_add_u32 v8, s9, v1\n\t"
    "v_mov_b32 v6, v2\n\t"
    "s_lshl_b32 s6, s59, 14\n\t"
    "v_add_u32 v2, s6, v1\n\t"
    "v_and_b32 v3, 0xf0, v1\n\t"
    "v_lshrrev_b32 v4, 5, v0\n\t"
    "v_lshl_add_u32 v3, v4, 8, v3\n\t"
    "s_add_u32 s6, s6, 2048\n\t"
    "v_add_u32 v3, s6, v3\n\t"
    "s_lshl_b32 s6, s59, 6\n\t"
    "s_add_u32 s6, s6, 65536\n\t"
    "v_mov_b32 v4, s6\n\t"
    "s_mov_b32 s76, 0x8000\n\t"
    "s_mov_b32 s7, 524288\n\t"
    "s_setprio 2\n\t"
    ".LX17_%=:\n\t"
    "s_waitcnt vmcnt(0) lgkmcnt(0)\n\t"
    "v_add3_u32 v37, v64, v65, v66\n\t"
    "v_add3_u32 v37, v37, v67, s56\n\t"
    "v_xor_b32 v36, v37, v80\n\t"
    "v_add3_u32 v37, v68, v69, v70\n\t"
    "v_add3_u32 v37, v37, v71, s56\n\t"
    "v_bitop3_b32 v36, v36, v37, v81 bitop3:0xf6\n\t"
    "v_add3_u32 v37, v72, v73, v74\n\t"
    "v_add3_u32 v37, v37, v75, s56\n\t"
    "v_bitop3_b32 v36, v36, v37, v82 bitop3:0xf6\n\t"
    "v_add3_u32 v37, v76, v77, v78\n\t"
    "v_add3_u32 v37, v37, v79, s56\n\t"
    "v_bitop3_b32 v36, v36, v37, v83 bitop3:0xf6\n\t"
    "v_cmp_ne_u32 vcc, 0, v36\n\t"
    "s_cbranch_vccz .LX18_%=\n\t"
    "s_sub_u32 s7, s7, 1\n\t"
    "s_cmp_eq_u32 s7, 0\n\t"
    "s_cbranch_scc1 .LX18_%=\n\t"
    "buffer_load_dwordx4 v[64:67], v1, s[36:39], s8 offen offset:0 sc1\n\t"
    "buffer_load_dwordx4 v[68:71], v1, s[36:39], s8 offen offset:1024 sc1\n\t"
    "buffer_load_dwordx4 v[72:75], v1, s[36:39], s8 offen offset:2048 sc1\n\t"
    "buffer_load_dwordx4 v[76:79], v1, s[36:39], s8 offen offset:3072 sc1\n\t"
    "buffer_load_dwordx4 v[80:83], v1, s[36:39], s10 offen sc1\n\t"
    "s_branch .LX17_%=\n\t"
    ".LX18_%=:\n\t"
    "ds_write_b128 v8, v[64:67] offset:0\n\t"
    "ds_write_b128 v8, v[68:71] offset:1024\n\t"
    "ds_write_b128 v8, v[72:75] offset:2048\n\t"
    "ds_write_b128 v8, v[76:79] offset:3072\n\t"
    "s_barrier\n\t"
    "ds_read_b128 a[32:35], v7\n\t"
    ".LXp174_%=:\n\t"
    "s_cbranch_execz .LXp182_%=\n\t"
    "ds_read_b128 a[36:39], v5\n\t"
    "v_xor_b32 v7, 0x400, v9\n\t"
    "s_waitcnt lgkmcnt(1)\n\t"
    "v_mfma_f32_32x32x16_bf16 v[16:31], v[64:67], a[32:35], 0\n\t"
    "s_nop 7\n\t"
    "s_nop 3\n\t"
    "v_max3_f32 v36, v16, v17, v18\n\t"
    "v_max3_f32 v36, v36, v19, v20\n\t"
    "v_max3_f32 v36, v36, v21, v22\n\t"
    "v_max3_f32 v36, v36, v23, v24\n\t"
    "v_mov_b32 v37, v36\n\t"
    "s_mov_b32 s8, 4\n\t"
    "s_nop 0\n\t"
    "v_permlane32_swap_b32 v36, v37\n\t"
    "v_min_f32 v48, -v36, -v37\n\t"
    "ds_write_b32 v9, v48\n\t"
    "v_mov_b32 v49, v48\n\t"
    "v_mov_b64 v[50:51], v[48:49]\n\t"
    "v_mov_b64 v[52:53], v[48:49]\n\t"
    "v_mov_b64 v[54:55], v[48:49]\n\t"
    "v_mov_b64 v[56:57], v[48:49]\n\t"
    "v_mov_b64 v[58:59], v[48:49]\n\t"
    "v_mov_b64 v[60:61], v[48:49]\n\t"
    "v_mov_b64 v[62:63], v[48:49]\n\t"
    "s_waitcnt lgkmcnt(0)\n\t"
    "s_barrier\n\t"
    "ds_read_b128 a[40:43], v2 offset:0\n\t"
    "ds_read_b32 v64, v7\n\t"
    "s_mov_b64 exec, s[60:61]\n\t"
    "ds_read_b128 a[48:51], v3 offset:0\n\t"
    "ds_read_b128 a[52:55], v3 offset:512\n\t"
    "s_mov_b64 exec, -1\n\t"
    "s_nop 2\n\t"
    "s_waitcnt lgkmcnt(3)\n\t"
    "v_mfma_f32_32x32x16_bf16 v[16:31], a[40:43], a[32:35], v[48:63]\n\t"
    "s_waitcnt lgkmcnt(2)\n\t"
    "v_mov_b32 v65, v64\n\t"
    "v_mov_b64 v[66:67], v[64:65]\n\t"
    "v_mov_b64 v[68:69], v[64:65]\n\t"
    "v_mov_b64 v[70:71], v[64:65]\n\t"
    "v_mov_b64 v[72:73], v[64:65]\n\t"
    "v_mov_b64 v[74:75], v[64:65]\n\t"
    "v_mov_b64 v[76:77], v[64:65]\n\t"
    "v_mov_b64 v[78:79], v[64:65]\n\t"
    "s_waitcnt lgkmcnt(0)\n\t"
    "s_nop 2\n\t"
    "v_exp_f32 v16, v16\n\t"
    "v_exp_f32 v17, v17\n\t"
    "v_exp_f32 v18, v18\n\t"
    "v_exp_f32 v19, v19\n\t"
    "v_exp_f32 v20, v20\n\t"
    "v_exp_f32 v21, v21\n\t"
    "v_mfma_f32_32x32x16_bf16 v[32:47], a[40:43], a[36:39], v[64:79]\n\t"
    "v_exp_f32 v22, v22\n\t"
    "v_exp_f32 v23, v23\n\t"
    "v_exp_f32 v24, v24\n\t"
    "ds_read_b128 a[44:47], v2 offset:1024\n\t"
    "v_exp_f32 v25, v25\n\t"
    "v_smfmac_f32_32x32x32_bf16 a[0:15], a[48:51], v[16:23], v12\n\t"
    "v_exp_f32 v26, v26\n\t"
    "v_exp_f32 v27, v27\n\t"
    "v_exp_f32 v28, v28\n\t"
    "s_mov_b64 exec, s[60:61]\n\t"
    "ds_read_b128 a[56:59], v3 offset:1024\n\t"
    "ds_read_b128 a[60:63], v3 offset:1536\n\t"
    "s_mov_b64 exec, -1\n\t"
    "v_exp_f32 v29, v29\n\t"
    "v_exp_f32 v30, v30\n\t"
    "v_exp_f32 v31, v31\n\t"
    "v_exp_f32 v32, v32\n\t"
    "v_exp_f32 v33, v33\n\t"
    "v_smfmac_f32_32x32x32_bf16 a[0:15], a[52:55], v[24:31], v12\n\t"
    "v_exp_f32 v34, v34\n\t"
    "v_exp_f32 v35, v35\n\t"
    "v_exp_f32 v36, v36\n\t"
    "s_waitcnt lgkmcnt(0)\n\t"
    "v_mfma_f32_32x32x16_bf16 v[16:31], a[44:47], a[32:35], v[48:63]\n\t"
    "v_exp_f32 v37, v37\n\t"
    "v_exp_f32 v38, v38\n\t"
    "v_exp_f32 v39, v39\n\t"
    "v_exp_f32 v40, v40\n\t"
    "v_exp_f32 v41, v41\n\t"
    "v_smfmac_f32_32x32x32_bf16 a[16:31], a[48:51], v[32:39], v12\n\t"
    "v_exp_f32 v42, v42\n\t"
    "v_exp_f32 v43, v43\n\t"
    "v_exp_f32 v44, v44\n\t"
    "v_exp_f32 v45, v45\n\t"
    "v_exp_f32 v46, v46\n\t"
    "v_exp_f32 v47, v47\n\t"
    "v_exp_f32 v16, v16\n\t"
    "v_exp_f32 v17, v17\n\t"
    "v_smfmac_f32_32x32x32_bf16 a[16:31], a[52:55], v[40:47], v12\n\t"
    "v_exp_f32 v18, v18\n\t"
    "v_exp_f32 v19, v19\n\t"
    "v_exp_f32 v20, v20\n\t"
    "v_exp_f32 v21, v21\n\t"
    "v_mfma_f32_32x32x16_bf16 v[32:47], a[44:47], a[36:39], v[64:79]\n\t"
    "v_exp_f32 v22, v22\n\t"
    "v_exp_f32 v23, v23\n\t"
    "v_exp_f32 v24, v24\n\t"
    "ds_read_b128 a[40:43], v2 offset:4096\n\t"
    "v_exp_f32 v25, v25\n\t"
    "v_smfmac_f32_32x32x32_bf16 a[0:15], a[56:59], v[16:23], v12\n\t"
    "v_exp_f32 v26, v26\n\t"
    "v_exp_f32 v27, v27\n\t"
    "v_exp_f32 v28, v28\n\t"
    "s_mov_b64 exec, s[60:61]\n\t"
    "ds_read_b128 a[48:51], v3 offset:4096\n\t"
    "ds_read_b128 a[52:55], v3 offset:4608\n\t"
    "s_mov_b64 exec, -1\n\t"
    "v_exp_f32 v29, v29\n\t"
    "v_exp_f32 v30, v30\n\t"
    "v_exp_f32 v31, v31\n\t"
    "v_exp_f32 v32, v32\n\t"
    "v_exp_f32 v33, v33\n\t"
    "v_smfmac_f32_32x32x32_bf16 a[0:15], a[60:63], v[24:31], v12\n\t"
    "v_exp_f32 v34, v34\n\t"
    "v_exp_f32 v35, v35\n\t"
    "v_exp_f32 v36, v36\n\t"
    "s_waitcnt lgkmcnt(0)\n\t"
    "v_mfma_f32_32x32x16_bf16 v[16:31], a[40:43], a[32:35], v[48:63]\n\t"
    "v_exp_f32 v37, v37\n\t"
    "v_exp_f32 v38, v38\n\t"
    "v_exp_f32 v39, v39\n\t"
    "v_exp_f32 v40, v40\n\t"
    "v_exp_f32 v41, v41\n\t"
    "v_smfmac_f32_32x32x32_bf16 a[16:31], a[56:59], v[32:39], v12\n\t"
    "v_exp_f32 v42, v42\n\t"
    "v_exp_f32 v43, v43\n\t"
    "v_exp_f32 v44, v44\n\t"
    "v_exp_f32 v45, v45\n\t"
    "v_exp_f32 v46, v46\n\t"
    "v_exp_f32 v47, v47\n\t"
    "v_exp_f32 v16, v16\n\t"
    "v_exp_f32 v17, v17\n\t"
    "v_smfmac_f32_32x32x32_bf16 a[16:31], a[60:63], v[40:47], v12\n\t"
    "v_exp_f32 v18, v18\n\t"
    "v_exp_f32 v19, v19\n\t"
    "v_exp_f32 v20, v20\n\t"
    "v_exp_f32 v21, v21\n\t"
    "v_mfma_f32_32x32x16_bf16 v[32:47], a[40:43], a[36:39], v[64:79]\n\t"
    "v_exp_f32 v22, v22\n\t"
    "v_exp_f32 v23, v23\n\t"
    "v_exp_f32 v24, v24\n\t"
    "ds_read_b128 a[44:47], v2 offset:5120\n\t"
    "v_exp_f32 v25, v25\n\t"
    "v_smfmac_f32_32x32x32_bf16 a[0:15], a[48:51], v[16:23], v12\n\t"
    "v_exp_f32 v26, v26\n\t"
    "v_exp_f32 v27, v27\n\t"
    "v_exp_f32 v28, v28\n\t"
    "s_mov_b64 exec, s[60:61]\n\t"
    "ds_read_b128 a[56:59], v3 offset:5120\n\t"
    "ds_read_b128 a[60:63], v3 offset:5632\n\t"
    "s_mov_b64 exec, -1\n\t"
    "v_exp_f32 v29, v29\n\t"
    "v_exp_f32 v30, v30\n\t"
    "v_exp_f32 v31, v31\n\t"
    "v_exp_f32 v32, v32\n\t"
    "v_exp_f32 v33, v33\n\t"
    "v_smfmac_f32_32x32x32_bf16 a[0:15], a[52:55], v[24:31], v12\n\t"
    "v_exp_f32 v34, v34\n\t"
    "v_exp_f32 v35, v35\n\t"
    "v_exp_f32 v36, v36\n\t"
    "s_waitcnt lgkmcnt(0)\n\t"
    "v_mfma_f32_32x32x16_bf16 v[16:31], a[44:47], a[32:35], v[48:63]\n\t"
    "v_exp_f32 v37, v37\n\t"
    "v_exp_f32 v38, v38\n\t"
    "v_exp_f32 v39, v39\n\t"
    "v_exp_f32 v40, v40\n\t"
    "v_exp_f32 v41, v41\n\t"
    "v_smfmac_f32_32x32x32_bf16 a[16:31], a[48:51], v[32:39], v12\n\t"
    "v_exp_f32 v42, v42\n\t"
    "v_exp_f32 v43, v43\n\t"
    "v_exp_f32 v44, v44\n\t"
    "v_exp_f32 v45, v45\n\t"
    "v_exp_f32 v46, v46\n\t"
    "v_exp_f32 v47, v47\n\t"
    "v_exp_f32 v16, v16\n\t"
    "v_exp_f32 v17, v17\n\t"
    "v_smfmac_f32_32x32x32_bf16 a[16:31], a[52:55], v[40:47], v12\n\t"
    "v_exp_f32 v18, v18\n\t"
    "v_exp_f32 v19, v19\n\t"
    "v_exp_f32 v20, v20\n\t"
    "v_exp_f32 v21, v21\n\t"
    "v_mfma_f32_32x32x16_bf16 v[32:47], a[44:47], a[36:39], v[64:79]\n\t"
    "v_exp_f32 v22, v22\n\t"
    "v_exp_f32 v23, v23\n\t"
    "v_exp_f32 v24, v24\n\t"
    "ds_read_b128 a[40:43], v2 offset:8192\n\t"
    "v_exp_f32 v25, v25\n\t"
    "v_smfmac_f32_32x32x32_bf16 a[0:15], a[56:59], v[16:23], v12\n\t"
    "v_exp_f32 v26, v26\n\t"
    "v_exp_f32 v27, v27\n\t"
    "v_exp_f32 v28, v28\n\t"
    "s_mov_b64 exec, s[60:61]\n\t"
    "ds_read_b128 a[48:51], v3 offset:8192\n\t"
    "ds_read_b128 a[52:55], v3 offset:8704\n\t"
    "s_mov_b64 exec, -1\n\t"
    "v_exp_f32 v29, v29\n\t"
    "v_exp_f32 v30, v30\n\t"
    "v_exp_f32 v31, v31\n\t"
    "v_exp_f32 v32, v32\n\t"
    "v_exp_f32 v33, v33\n\t"
    "v_smfmac_f32_32x32x32_bf16 a[0:15], a[60:63], v[24:31], v12\n\t"
    "v_exp_f32 v34, v34\n\t"
    "v_exp_f32 v35, v35\n\t"
    "v_exp_f32 v36, v36\n\t"
    "s_waitcnt lgkmcnt(0)\n\t"
    "v_mfma_f32_32x32x16_bf16 v[16:31], a[40:43], a[32:35], v[48:63]\n\t"
    "v_exp_f32 v37, v37\n\t"
    "v_exp_f32 v38, v38\n\t"
    "v_exp_f32 v39, v39\n\t"
    "v_exp_f32 v40, v40\n\t"
    "v_exp_f32 v41, v41\n\t"
    "v_smfmac_f32_32x32x32_bf16 a[16:31], a[56:59], v[32:39], v12\n\t"
    "v_exp_f32 v42, v42\n\t"
    "v_exp_f32 v43, v43\n\t"
    "v_exp_f32 v44, v44\n\t"
    "v_exp_f32 v45, v45\n\t"
    "v_exp_f32 v46, v46\n\t"
    "v_exp_f32 v47, v47\n\t"
    "v_exp_f32 v16, v16\n\t"
    "v_exp_f32 v17, v17\n\t"
    "v_smfmac_f32_32x32x32_bf16 a[16:31], a[60:63], v[40:47], v12\n\t"
    "v_exp_f32 v18, v18\n\t"
    "v_exp_f32 v19, v19\n\t"
    "v_exp_f32 v20, v20\n\t"
    "v_exp_f32 v21, v21\n\t"
    "v_mfma_f32_32x32x16_bf16 v[32:47], a[40:43], a[36:39], v[64:79]\n\t"
    "v_exp_f32 v22, v22\n\t"
    "v_exp_f32 v23, v23\n\t"
    "v_exp_f32 v24, v24\n\t"
    "ds_read_b128 a[44:47], v2 offset:9216\n\t"
    "v_exp_f32 v25, v25\n\t"
    "v_smfmac_f32_32x32x32_bf16 a[0:15], a[48:51], v[16:23], v12\n\t"
    "v_exp_f32 v26, v26\n\t"
    "v_exp_f32 v27, v27\n\t"
    "v_exp_f32 v28, v28\n\t"
    "s_mov_b64 exec, s[60:61]\n\t"
    "ds_read_b128 a[56:59], v3 offset:9216\n\t"
    "ds_read_b128 a[60:63], v3 offset:9728\n\t"
    "s_mov_b64 exec, -1\n\t"
    "v_exp_f32 v29, v29\n\t"
    "v_exp_f32 v30, v30\n\t"
    "v_exp_f32 v31, v31\n\t"
    "v_exp_f32 v32, v32\n\t"
    "v_exp_f32 v33, v33\n\t"
    "v_smfmac_f32_32x32x32_bf16 a[0:15], a[52:55], v[24:31], v12\n\t"
    "v_exp_f32 v34, v34\n\t"
    "v_exp_f32 v35, v35\n\t"
    "v_exp_f32 v36, v36\n\t"
    "s_waitcnt lgkmcnt(0)\n\t"
    "v_mfma_f32_32x32x16_bf16 v[16:31], a[44:47], a[32:35], v[48:63]\n\t"
    "v_exp_f32 v37, v37\n\t"
    "v_exp_f32 v38, v38\n\t"
    "v_exp_f32 v39, v39\n\t"
    "v_exp_f32 v40, v40\n\t"
    "v_exp_f32 v41, v41\n\t"
    "v_smfmac_f32_32x32x32_bf16 a[16:31], a[48:51], v[32:39], v12\n\t"
    "v_exp_f32 v42, v42\n\t"
    "v_exp_f32 v43, v43\n\t"
    "v_exp_f32 v44, v44\n\t"
    "v_exp_f32 v45, v45\n\t"
    "v_exp_f32 v46, v46\n\t"
    "v_exp_f32 v47, v47\n\t"
    "s_setprio 1\n\t"
    "v_exp_f32 v16, v16\n\t"
    "v_exp_f32 v17, v17\n\t"
    "v_smfmac_f32_32x32x32_bf16 a[16:31], a[52:55], v[40:47], v12\n\t"
    "v_exp_f32 v18, v18\n\t"
    "v_exp_f32 v19, v19\n\t"
    "v_exp_f32 v20, v20\n\t"
    "v_exp_f32 v21, v21\n\t"
    "v_mfma_f32_32x32x16_bf16 v[32:47], a[44:47], a[36:39], v[64:79]\n\t"
    "v_exp_f32 v22, v22\n\t"
    "v_exp_f32 v23, v23\n\t"
    "v_exp_f32 v24, v24\n\t"
    "ds_read_b128 a[40:43], v2 offset:12288\n\t"
    "v_exp_f32 v25, v25\n\t"
    "v_smfmac_f32_32x32x32_bf16 a[0:15], a[56:59], v[16:23], v12\n\t"
    "v_exp_f32 v26, v26\n\t"
    "v_exp_f32 v27, v27\n\t"
    "v_exp_f32 v28, v28\n\t"
    "s_mov_b64 exec, s[60:61]\n\t"
    "ds_read_b128 a[48:51], v3 offset:12288\n\t"
    "ds_read_b128 a[52:55], v3 offset:12800\n\t"
    "s_mov_b64 exec, -1\n\t"
    "v_exp_f32 v29, v29\n\t"
    "v_exp_f32 v30, v30\n\t"
    "v_exp_f32 v31, v31\n\t"
    "v_exp_f32 v32, v32\n\t"
    "v_exp_f32 v33, v33\n\t"
    "v_smfmac_f32_32x32x32_bf16 a[0:15], a[60:63], v[24:31], v12\n\t"
    "v_exp_f32 v34, v34\n\t"
    "v_exp_f32 v35, v35\n\t"
    "v_exp_f32 v36, v36\n\t"
    "s_waitcnt lgkmcnt(0)\n\t"
    "v_mfma_f32_32x32x16_bf16 v[16:31], a[40:43], a[32:35], v[48:63]\n\t"
    "v_exp_f32 v37, v37\n\t"
    "v_exp_f32 v38, v38\n\t"
    "v_exp_f32 v39, v39\n\t"
    "v_exp_f32 v40, v40\n\t"
    "v_exp_f32 v41, v41\n\t"
    "v_smfmac_f32_32x32x32_bf16 a[16:31], a[56:59], v[32:39], v12\n\t"
    "v_exp_f32 v42, v42\n\t"
    "v_exp_f32 v43, v43\n\t"
    "v_exp_f32 v44, v44\n\t"
    "v_exp_f32 v45, v45\n\t"
    "v_exp_f32 v46, v46\n\t"
    "v_exp_f32 v47, v47\n\t"
    "v_exp_f32 v16, v16\n\t"
    "v_exp_f32 v17, v17\n\t"
    "v_smfmac_f32_32x32x32_bf16 a[16:31], a[60:63], v[40:47], v12\n\t"
    "v_exp_f32 v18, v18\n\t"
    "v_exp_f32 v19, v19\n\t"
    "v_exp_f32 v20, v20\n\t"
    "v_exp_f32 v21, v21\n\t"
    "v_mfma_f32_32x32x16_bf16 v[32:47], a[40:43], a[36:39], v[64:79]\n\t"
    "v_exp_f32 v22, v22\n\t"
    "v_exp_f32 v23, v23\n\t"
    "v_exp_f32 v24, v24\n\t"
    "ds_read_b128 a[44:47], v2 offset:13312\n\t"
    "v_exp_f32 v25, v25\n\t"
    "v_smfmac_f32_32x32x32_bf16 a[0:15], a[48:51], v[16:23], v12\n\t"
    "v_exp_f32 v26, v26\n\t"
    "v_exp_f32 v27, v27\n\t"
    "v_exp_f32 v28, v28\n\t"
    "s_mov_b64 exec, s[60:61]\n\t"
    "ds_read_b128 a[56:59], v3 offset:13312\n\t"
    "ds_read_b128 a[60:63], v3 offset:13824\n\t"
    "s_mov_b64 exec, -1\n\t"
    "v_exp_f32 v29, v29\n\t"
    "v_exp_f32 v30, v30\n\t"
    "v_exp_f32 v31, v31\n\t"
    "v_exp_f32 v32, v32\n\t"
    "ds_read_b32 v14, v4 offset:4\n\t"
    "v_exp_f32 v33, v33\n\t"
    "v_smfmac_f32_32x32x32_bf16 a[0:15], a[52:55], v[24:31], v12\n\t"
    "v_exp_f32 v34, v34\n\t"
    "v_exp_f32 v35, v35\n\t"
    "v_exp_f32 v36, v36\n\t"
    "s_waitcnt lgkmcnt(0)\n\t"
    "v_mfma_f32_32x32x16_bf16 v[16:31], a[44:47], a[32:35], v[48:63]\n\t"
    "v_exp_f32 v37, v37\n\t"
    "v_exp_f32 v38, v38\n\t"
    "v_exp_f32 v39, v39\n\t"
    "v_exp_f32 v40, v40\n\t"
    "v_exp_f32 v41, v41\n\t"
    "v_smfmac_f32_32x32x32_bf16 a[16:31], a[48:51], v[32:39], v12\n\t"
    "v_exp_f32 v42, v42\n\t"
    "v_exp_f32 v43, v43\n\t"
    "v_exp_f32 v44, v44\n\t"
    "v_exp_f32 v45, v45\n\t"
    "v_exp_f32 v46, v46\n\t"
    "v_exp_f32 v47, v47\n\t"
    "v_readfirstlane_b32 s6, v14\n\t"
    "s_cmp_ge_u32 s6, 4\n\t"
    "s_cbranch_scc0 .LX28_%=\n\t"
    ".LX29_%=:\n\t"
    "v_exp_f32 v16, v16\n\t"
    "v_exp_f32 v17, v17\n\t"
    "v_smfmac_f32_32x32x32_bf16 a[16:31], a[52:55], v[40:47], v12\n\t"
    "v_exp_f32 v18, v18\n\t"
    "v_exp_f32 v19, v19\n\t"
    "v_exp_f32 v20, v20\n\t"
    "v_exp_f32 v21, v21\n\t"
    "v_mfma_f32_32x32x16_bf16 v[32:47], a[44:47], a[36:39], v[64:79]\n\t"
    "v_exp_f32 v22, v22\n\t"
    "v_exp_f32 v23, v23\n\t"
    "v_exp_f32 v24, v24\n\t"
    "ds_read_b128 a[40:43], v2 offset:32768\n\t"
    "v_exp_f32 v25, v25\n\t"
    "v_smfmac_f32_32x32x32_bf16 a[0:15], a[56:59], v[16:23], v12\n\t"
    "v_exp_f32 v26, v26\n\t"
    "v_exp_f32 v27, v27\n\t"
    "v_exp_f32 v28, v28\n\t"
    "s_mov_b64 exec, s[60:61]\n\t"
    "ds_read_b128 a[48:51], v3 offset:32768\n\t"
    "ds_read_b128 a[52:55], v3 offset:33280\n\t"
    "s_mov_b64 exec, 1\n\t"
    "ds_add_u32 v4, v13 offset:32\n\t"
    "s_mov_b64 exec, -1\n\t"
    "v_exp_f32 v29, v29\n\t"
    "v_exp_f32 v30, v30\n\t"
    "v_exp_f32 v31, v31\n\t"
    "v_exp_f32 v32, v32\n\t"
    "v_exp_f32 v33, v33\n\t"
    "v_smfmac_f32_32x32x32_bf16 a[0:15], a[60:63], v[24:31], v12\n\t"
    "v_exp_f32 v34, v34\n\t"
    "v_exp_f32 v35, v35\n\t"
    "v_exp_f32 v36, v36\n\t"
    "s_waitcnt lgkmcnt(0)\n\t"
    "v_mfma_f32_32x32x16_bf16 v[16:31], a[40:43], a[32:35], v[48:63]\n\t"
    "v_exp_f32 v37, v37\n\t"
    "v_exp_f32 v38, v38\n\t"
    "v_exp_f32 v39, v39\n\t"
    "v_exp_f32 v40, v40\n\t"
    "v_exp_f32 v41, v41\n\t"
    "v_smfmac_f32_32x32x32_bf16 a[16:31], a[56:59], v[32:39], v12\n\t"
    "v_exp_f32 v42, v42\n\t"
    "v_exp_f32 v43, v43\n\t"
    "v_exp_f32 v44, v44\n\t"
    "v_exp_f32 v45, v45\n\t"
    "v_exp_f32 v46, v46\n\t"
    "v_exp_f32 v47, v47\n\t"
    "s_barrier\n\t"
    "s_lshl_b32 s73, s4, 16\n\t"
    "s_lshl_b32 s6, s3, 13\n\t"
    "s_add_u32 s73, s73, s6\n\t"
    "s_mul_i32 s10, s3, 0x6000\n\t"
    "s_lshl_b32 s7, s5, 7\n\t"
    "s_add_u32 s10, s10, s7\n\t"
    "s_lshl_b32 s12, s5, 1\n\t"
    "s_add_u32 s12, s12, s4\n\t"
    "s_add_u32 s12, s12, 3\n\t"
    "s_mov_b32 s11, 0x20000\n\t"
    "s_mov_b32 s15, 0x22000\n\t"
    "s_mov_b32 s14, 4\n\t"
    "s_mov_b32 s63, 0\n\t"
    "v_xor_b32 v9, 32, v10\n\t"
    "s_and_b32 s7, s12, 7\n\t"
    "s_mul_i32 s7, s7, 0x30000\n\t"
    "s_add_u32 m0, s13, 0x3c00\n\t"
    "s_add_u32 s7, s7, s10\n\t"
    "buffer_load_dwordx4 v10, s[52:55], s7 offen offset:1024 lds\n\t"
    "s_add_u32 m0, m0, 0x1c00\n\t"
    "s_nop 0\n\t"
    "buffer_load_dwordx4 v9, s[52:55], s7 offen offset:2048 lds\n\t"
    ".LX103_%=:\n\t"
    "s_add_u32 m0, s13, s11\n\t"
    "s_and_b32 s6, s12, 7\n\t"
    "s_lshl_b32 s6, s6, 7\n\t"
    "s_add_u32 s6, s6, s73\n\t"
    "s_add_u32 s12, s12, 1\n\t"
    "s_and_b32 s7, s12, 7\n\t"
    "s_mul_i32 s7, s7, 0x30000\n\t"
    "s_add_u32 s7, s7, s10\n\t"
    "s_waitcnt vmcnt(7)\n\t"
    "s_barrier\n\t"
    "buffer_load_dwordx4 v6, s[48:51], s6 offen lds\n\t"
    "s_cmp_eq_u32 s14, 0\n\t"
    "s_cbranch_scc1 .LX104_%=\n\t"
    "s_add_u32 m0, s13, s15\n\t"
    "s_cmp_eq_u32 s11, 0\n\t"
    "buffer_load_dwordx4 v10, s[52:55], s7 offen lds\n\t"
    "s_cselect_b32 s11, 0x28000, s11\n\t"
    "s_add_u32 m0, m0, 0x1c00\n\t"
    "s_sub_u32 s11, s11, 0x8000\n\t"
    "buffer_load_dwordx4 v10, s[52:55], s7 offen offset:1024 lds\n\t"
    "s_add_u32 m0, m0, 0x1c00\n\t"
    "s_cmp_eq_u32 s11, 0x10000\n\t"
    "buffer_load_dwordx4 v9, s[52:55], s7 offen offset:2048 lds\n\t"
    "s_cselect_b32 s11, 0, s11\n\t"
    "s_cmp_eq_u32 s15, 0x2000\n\t"
    "s_cselect_b32 s15, 0x2a000, s15\n\t"
    "s_sub_u32 s15, s15, 0x8000\n\t"
    "s_cmp_eq_u32 s15, 0xa000\n\t"
    "s_cselect_b32 s15, 0x2000, s15\n\t"
    "s_sub_u32 s14, s14, 1\n\t"
    "s_branch .LX103_%=\n\t"
    ".LX104_%=:\n\t"
    "v_bfe_u32 v7, v0, 4, 1\n\t"
    "v_and_b32 v81, 0xf0, v1\n\t"
    "v_lshl_add_u32 v81, v7, 14, v81\n\t"
    "v_lshrrev_b32 v7, 5, v0\n\t"
    "v_lshl_add_u32 v81, v7, 8, v81\n\t"
    "s_and_b32 s6, s58, 1\n\t"
    "s_lshl_b32 s6, s6, 15\n\t"
    "s_lshl_b32 s7, s77, 16\n\t"
    "s_add_u32 s6, s6, s7\n\t"
    "s_lshl_b32 s7, s78, 11\n\t"
    "s_add_u32 s6, s6, s7\n\t"
    "s_lshr_b32 s7, s58, 1\n\t"
    "s_lshl_b32 s7, s7, 10\n\t"
    "s_add_u32 s6, s6, s7\n\t"
    "s_lshl_b32 s7, s59, 9\n\t"
    "s_add_u32 s6, s6, s7\n\t"
    "s_add_u32 s74, s6, 3145728\n\t"
    "s_xor_b32 s75, s74, 512\n\t"
    "s_lshl_b32 s6, s77, 2\n\t"
    "s_add_u32 s6, s6, s58\n\t"
    "s_lshl_b32 s6, s6, 1\n\t"
    "s_add_u32 s6, s6, s59\n\t"
    "s_lshl_b32 s6, s6, 13\n\t"
    "s_add_u32 s64, s6, 3145728\n\t"
    "s_add_u32 s65, s64, 0x1000\n\t"
    "s_mov_b32 s67, 524288\n\t"
    "s_xor_b32 s6, s59, 1\n\t"
    "s_lshl_b32 s6, s6, 15\n\t"
    "v_lshrrev_b32 v5, 5, v0\n\t"
    "v_lshlrev_b32 v5, 4, v5\n\t"
    "v_bfe_u32 v9, v0, 2, 3\n\t"
    "v_add_u32 v5, v5, v9\n\t"
    "v_lshlrev_b32 v5, 7, v5\n\t"
    "v_lshrrev_b32 v9, 1, v9\n\t"
    "v_lshl_add_u32 v5, v9, 5, v5\n\t"
    "v_and_b32 v9, 3, v0\n\t"
    "v_lshl_add_u32 v5, v9, 3, v5\n\t"
    "v_add_u32 v5, s6, v5\n\t"
    "s_waitcnt vmcnt(4)\n\t"
    "s_barrier\n\t"
    "v_and_b32 v7, 15, v0\n\t"
    "v_lshlrev_b32 v7, 11, v7\n\t"
    "v_bfe_u32 v9, v0, 4, 1\n\t"
    "v_lshl_add_u32 v7, v9, 5, v7\n\t"
    "v_lshrrev_b32 v9, 5, v0\n\t"
    "v_lshl_add_u32 v7, v9, 4, v7\n\t"
    "s_lshl_b32 s6, s77, 2\n\t"
    "s_add_u32 s6, s6, s58\n\t"
    "s_lshl_b32 s6, s6, 15\n\t"
    "s_lshl_b32 s7, s78, 7\n\t"
    "s_add_u32 s6, s6, s7\n\t"
    "v_add_u32 v7, s6, v7\n\t"
    "s_lshl_b32 s6, s58, 2\n\t"
    "s_add_u32 s6, s6, 65536\n\t"
    "v_mov_b32 v6, s6\n\t"
    "v_mov_b32 v10, 65536\n\t"
    "s_add_u32 s9, s9, 0x20000\n\t"
    "v_add_u32 v8, s9, v1\n\t"
    "v_xor_b32 v11, 0x4000, v8\n\t"
    "v_and_b32 v9, 31, v0\n\t"
    "s_xor_b32 s6, s3, 4\n\t"
    "s_lshl_b32 s6, s6, 8\n\t"
    "s_add_u32 s6, s6, 67584\n\t"
    "v_lshl_add_u32 v9, v9, 2, s6\n\t"
    "s_waitcnt vmcnt(0)\n\t"
    "s_barrier\n\t"
    "s_setprio 2\n\t"
    "v_exp_f32 v16, v16\n\t"
    "v_exp_f32 v17, v17\n\t"
    "v_smfmac_f32_32x32x32_bf16 a[16:31], a[60:63], v[40:47], v12\n\t"
    "v_exp_f32 v18, v18\n\t"
    "v_exp_f32 v19, v19\n\t"
    "v_exp_f32 v20, v20\n\t"
    "v_exp_f32 v21, v21\n\t"
    "v_mfma_f32_32x32x16_bf16 v[32:47], a[40:43], a[36:39], v[64:79]\n\t"
    "v_exp_f32 v22, v22\n\t"
    "v_exp_f32 v23, v23\n\t"
    "v_exp_f32 v24, v24\n\t"
    "ds_read_b128 a[44:47], v2 offset:33792\n\t"
    "v_exp_f32 v25, v25\n\t"
    "v_smfmac_f32_32x32x32_bf16 a[0:15], a[48:51], v[16:23], v12\n\t"
    "v_exp_f32 v26, v26\n\t"
    "v_exp_f32 v27, v27\n\t"
    "v_exp_f32 v28, v28\n\t"
    "s_mov_b64 exec, s[60:61]\n\t"
    "ds_read_b128 a[56:59], v3 offset:33792\n\t"
    "ds_read_b128 a[60:63], v3 offset:34304\n\t"
    "s_mov_b64 exec, -1\n\t"
    "v_exp_f32 v29, v29\n\t"
    "v_exp_f32 v30, v30\n\t"
    "v_exp_f32 v31, v31\n\t"
    "v_exp_f32 v32, v32\n\t"
    "v_exp_f32 v33, v33\n\t"
    "v_smfmac_f32_32x32x32_bf16 a[0:15], a[52:55], v[24:31], v12\n\t"
    "v_exp_f32 v34, v34\n\t"
    "v_exp_f32 v35, v35\n\t"
    "v_exp_f32 v36, v36\n\t"
    "s_waitcnt lgkmcnt(0)\n\t"
    "v_mfma_f32_32x32x16_bf16 v[16:31], a[44:47], a[32:35], v[48:63]\n\t"
    "v_exp_f32 v37, v37\n\t"
    "v_exp_f32 v38, v38\n\t"
    "v_exp_f32 v39, v39\n\t"
    "v_exp_f32 v40, v40\n\t"
    "v_exp_f32 v41, v41\n\t"
    "v_smfmac_f32_32x32x32_bf16 a[16:31], a[48:51], v[32:39], v12\n\t"
    "v_exp_f32 v42, v42\n\t"
    "v_exp_f32 v43, v43\n\t"
    "v_exp_f32 v44, v44\n\t"
    "v_exp_f32 v45, v45\n\t"
    "v_exp_f32 v46, v46\n\t"
    "v_exp_f32 v47, v47\n\t"
    "v_exp_f32 v16, v16\n\t"
    "v_exp_f32 v17, v17\n\t"
    "v_smfmac_f32_32x32x32_bf16 a[16:31], a[52:55], v[40:47], v12\n\t"
    "v_exp_f32 v18, v18\n\t"
    "v_exp_f32 v19, v19\n\t"
    "v_exp_f32 v20, v20\n\t"
    "v_exp_f32 v21, v21\n\t"
    "v_mfma_f32_32x32x16_bf16 v[32:47], a[44:47], a[36:39], v[64:79]\n\t"
    "v_exp_f32 v22, v22\n\t"
    "v_exp_f32 v23, v23\n\t"
    "v_exp_f32 v24, v24\n\t"
    "ds_read_b128 a[40:43], v2 offset:36864\n\t"
    "v_exp_f32 v25, v25\n\t"
    "v_smfmac_f32_32x32x32_bf16 a[0:15], a[56:59], v[16:23], v12\n\t"
    "v_exp_f32 v26, v26\n\t"
    "v_exp_f32 v27, v27\n\t"
    "v_exp_f32 v28, v28\n\t"
    "s_mov_b64 exec, s[60:61]\n\t"
    "ds_read_b128 a[48:51], v3 offset:36864\n\t"
    "ds_read_b128 a[52:55], v3 offset:37376\n\t"
    "s_mov_b64 exec, -1\n\t"
    "v_exp_f32 v29, v29\n\t"
    "v_exp_f32 v30, v30\n\t"
    "v_exp_f32 v31, v31\n\t"
    "v_exp_f32 v32, v32\n\t"
    "v_exp_f32 v33, v33\n\t"
    "v_smfmac_f32_32x32x32_bf16 a[0:15], a[60:63], v[24:31], v12\n\t"
    "v_exp_f32 v34, v34\n\t"
    "v_exp_f32 v35, v35\n\t"
    "v_exp_f32 v36, v36\n\t"
    "s_waitcnt lgkmcnt(0)\n\t"
    "v_mfma_f32_32x32x16_bf16 v[16:31], a[40:43], a[32:35], v[48:63]\n\t"
    "v_exp_f32 v37, v37\n\t"
    "v_exp_f32 v38, v38\n\t"
    "v_exp_f32 v39, v39\n\t"
    "v_exp_f32 v40, v40\n\t"
    "v_exp_f32 v41, v41\n\t"
    "v_smfmac_f32_32x32x32_bf16 a[16:31], a[56:59], v[32:39], v12\n\t"
    "v_exp_f32 v42, v42\n\t"
    "v_exp_f32 v43, v43\n\t"
    "v_exp_f32 v44, v44\n\t"
    "v_exp_f32 v45, v45\n\t"
    "v_exp_f32 v46, v46\n\t"
    "v_exp_f32 v47, v47\n\t"
    "v_exp_f32 v16, v16\n\t"
    "v_exp_f32 v17, v17\n\t"
    "v_smfmac_f32_32x32x32_bf16 a[16:31], a[60:63], v[40:47], v12\n\t"
    "v_exp_f32 v18, v18\n\t"
    "v_exp_f32 v19, v19\n\t"
    "v_exp_f32 v20, v20\n\t"
    "v_exp_f32 v21, v21\n\t"
    "v_mfma_f32_32x32x16_bf16 v[32:47], a[40:43], a[36:39], v[64:79]\n\t"
    "v_exp_f32 v22, v22\n\t"
    "v_exp_f32 v23, v23\n\t"
    "v_exp_f32 v24, v24\n\t"
    "ds_read_b128 a[44:47], v2 offset:37888\n\t"
    "v_exp_f32 v25, v25\n\t"
    "v_smfmac_f32_32x32x32_bf16 a[0:15], a[48:51], v[16:23], v12\n\t"
    "v_exp_f32 v26, v26\n\t"
    "v_exp_f32 v27, v27\n\t"
    "v_exp_f32 v28, v28\n\t"
    "s_mov_b64 exec, s[60:61]\n\t"
    "ds_read_b128 a[56:59], v3 offset:37888\n\t"
    "ds_read_b128 a[60:63], v3 offset:38400\n\t"
    "s_mov_b64 exec, -1\n\t"
    "v_exp_f32 v29, v29\n\t"
    "v_exp_f32 v30, v30\n\t"
    "v_exp_f32 v31, v31\n\t"
    "v_exp_f32 v32, v32\n\t"
    "v_exp_f32 v33, v33\n\t"
    "v_smfmac_f32_32x32x32_bf16 a[0:15], a[52:55], v[24:31], v12\n\t"
    "v_exp_f32 v34, v34\n\t"
    "v_exp_f32 v35, v35\n\t"
    "v_exp_f32 v36, v36\n\t"
    "s_waitcnt lgkmcnt(0)\n\t"
    "v_mfma_f32_32x32x16_bf16 v[16:31], a[44:47], a[32:35], v[48:63]\n\t"
    "v_exp_f32 v37, v37\n\t"
    "v_exp_f32 v38, v38\n\t"
    "v_exp_f32 v39, v39\n\t"
    "v_exp_f32 v40, v40\n\t"
    "v_exp_f32 v41, v41\n\t"
    "v_smfmac_f32_32x32x32_bf16 a[16:31], a[48:51], v[32:39], v12\n\t"
    "v_exp_f32 v42, v42\n\t"
    "v_exp_f32 v43, v43\n\t"
    "v_exp_f32 v44, v44\n\t"
    "v_exp_f32 v45, v45\n\t"
    "v_exp_f32 v46, v46\n\t"
    "v_exp_f32 v47, v47\n\t"
    "v_exp_f32 v16, v16\n\t"
    "v_exp_f32 v17, v17\n\t"
    "v_smfmac_f32_32x32x32_bf16 a[16:31], a[52:55], v[40:47], v12\n\t"
    "v_exp_f32 v18, v18\n\t"
    "v_exp_f32 v19, v19\n\t"
    "v_exp_f32 v20, v20\n\t"
    "v_exp_f32 v21, v21\n\t"
    "v_mfma_f32_32x32x16_bf16 v[32:47], a[44:47], a[36:39], v[64:79]\n\t"
    "v_exp_f32 v22, v22\n\t"
    "v_exp_f32 v23, v23\n\t"
    "v_exp_f32 v24, v24\n\t"
    "ds_read_b128 a[40:43], v2 offset:40960\n\t"
    "v_exp_f32 v25, v25\n\t"
    "v_smfmac_f32_32x32x32_bf16 a[0:15], a[56:59], v[16:23], v12\n\t"
    "v_exp_f32 v26, v26\n\t"
    "v_exp_f32 v27, v27\n\t"
    "v_exp_f32 v28, v28\n\t"
    "s_mov_b64 exec, s[60:61]\n\t"
    "ds_read_b128 a[48:51], v3 offset:40960\n\t"
    "ds_read_b128 a[52:55], v3 offset:41472\n\t"
    "s_mov_b64 exec, -1\n\t"
    "v_exp_f32 v29, v29\n\t"
    "v_exp_f32 v30, v30\n\t"
    "v_exp_f32 v31, v31\n\t"
    "v_exp_f32 v32, v32\n\t"
    "v_exp_f32 v33, v33\n\t"
    "v_smfmac_f32_32x32x32_bf16 a[0:15], a[60:63], v[24:31], v12\n\t"
    "v_exp_f32 v34, v34\n\t"
    "v_exp_f32 v35, v35\n\t"
    "v_exp_f32 v36, v36\n\t"
    "s_waitcnt lgkmcnt(0)\n\t"
    "v_mfma_f32_32x32x16_bf16 v[16:31], a[40:43], a[32:35], v[48:63]\n\t"
    "v_exp_f32 v37, v37\n\t"
    "v_exp_f32 v38, v38\n\t"
    "v_exp_f32 v39, v39\n\t"
    "v_exp_f32 v40, v40\n\t"
    "v_exp_f32 v41, v41\n\t"
    "v_smfmac_f32_32x32x32_bf16 a[16:31], a[56:59], v[32:39], v12\n\t"
    "v_exp_f32 v42, v42\n\t"
    "v_exp_f32 v43, v43\n\t"
    "v_exp_f32 v44, v44\n\t"
    "v_exp_f32 v45, v45\n\t"
    "v_exp_f32 v46, v46\n\t"
    "v_exp_f32 v47, v47\n\t"
    "v_exp_f32 v16, v16\n\t"
    "v_exp_f32 v17, v17\n\t"
    "v_smfmac_f32_32x32x32_bf16 a[16:31], a[60:63], v[40:47], v12\n\t"
    "v_exp_f32 v18, v18\n\t"
    "v_exp_f32 v19, v19\n\t"
    "v_exp_f32 v20, v20\n\t"
    "v_exp_f32 v21, v21\n\t"
    "v_mfma_f32_32x32x16_bf16 v[32:47], a[40:43], a[36:39], v[64:79]\n\t"
    "v_exp_f32 v22, v22\n\t"
    "v_exp_f32 v23, v23\n\t"
    "v_exp_f32 v24, v24\n\t"
    "ds_read_b128 a[44:47], v2 offset:41984\n\t"
    "v_exp_f32 v25, v25\n\t"
    "v_smfmac_f32_32x32x32_bf16 a[0:15], a[48:51], v[16:23], v12\n\t"
    "v_exp_f32 v26, v26\n\t"
    "v_exp_f32 v27, v27\n\t"
    "v_exp_f32 v28, v28\n\t"
    "s_mov_b64 exec, s[60:61]\n\t"
    "ds_read_b128 a[56:59], v3 offset:41984\n\t"
    "ds_read_b128 a[60:63], v3 offset:42496\n\t"
    "s_mov_b64 exec, -1\n\t"
    "v_exp_f32 v29, v29\n\t"
    "v_exp_f32 v30, v30\n\t"
    "v_exp_f32 v31, v31\n\t"
    "v_exp_f32 v32, v32\n\t"
    "v_exp_f32 v33, v33\n\t"
    "v_smfmac_f32_32x32x32_bf16 a[0:15], a[52:55], v[24:31], v12\n\t"
    "v_exp_f32 v34, v34\n\t"
    "v_exp_f32 v35, v35\n\t"
    "v_exp_f32 v36, v36\n\t"
    "s_waitcnt lgkmcnt(0)\n\t"
    "v_mfma_f32_32x32x16_bf16 v[16:31], a[44:47], a[32:35], v[48:63]\n\t"
    "v_exp_f32 v37, v37\n\t"
    "v_exp_f32 v38, v38\n\t"
    "v_exp_f32 v39, v39\n\t"
    "v_exp_f32 v40, v40\n\t"
    "v_exp_f32 v41, v41\n\t"
    "v_smfmac_f32_32x32x32_bf16 a[16:31], a[48:51], v[32:39], v12\n\t"
    "v_exp_f32 v42, v42\n\t"
    "v_exp_f32 v43, v43\n\t"
    "v_exp_f32 v44, v44\n\t"
    "v_exp_f32 v45, v45\n\t"
    "v_exp_f32 v46, v46\n\t"
    "v_exp_f32 v47, v47\n\t"
    "v_exp_f32 v16, v16\n\t"
    "v_exp_f32 v17, v17\n\t"
    "v_smfmac_f32_32x32x32_bf16 a[16:31], a[52:55], v[40:47], v12\n\t"
    "v_exp_f32 v18, v18\n\t"
    "v_exp_f32 v19, v19\n\t"
    "v_exp_f32 v20, v20\n\t"
    "v_exp_f32 v21, v21\n\t"
    "v_mfma_f32_32x32x16_bf16 v[32:47], a[44:47], a[36:39], v[64:79]\n\t"
    "v_exp_f32 v22, v22\n\t"
    "v_exp_f32 v23, v23\n\t"
    "v_exp_f32 v24, v24\n\t"
    "ds_read_b128 a[40:43], v2 offset:45056\n\t"
    "v_exp_f32 v25, v25\n\t"
    "v_smfmac_f32_32x32x32_bf16 a[0:15], a[56:59], v[16:23], v12\n\t"
    "v_exp_f32 v26, v26\n\t"
    "v_exp_f32 v27, v27\n\t"
    "v_exp_f32 v28, v28\n\t"
    "s_mov_b64 exec, s[60:61]\n\t"
    "ds_read_b128 a[48:51], v3 offset:45056\n\t"
    "ds_read_b128 a[52:55], v3 offset:45568\n\t"
    "s_mov_b64 exec, -1\n\t"
    "v_exp_f32 v29, v29\n\t"
    "v_exp_f32 v30, v30\n\t"
    "v_exp_f32 v31, v31\n\t"
    "v_exp_f32 v32, v32\n\t"
    "v_exp_f32 v33, v33\n\t"
    "v_smfmac_f32_32x32x32_bf16 a[0:15], a[60:63], v[24:31], v12\n\t"
    "v_exp_f32 v34, v34\n\t"
    "v_exp_f32 v35, v35\n\t"
    "v_exp_f32 v36, v36\n\t"
    "s_waitcnt lgkmcnt(0)\n\t"
    "v_mfma_f32_32x32x16_bf16 v[16:31], a[40:43], a[32:35], v[48:63]\n\t"
    "v_exp_f32 v37, v37\n\t"
    "v_exp_f32 v38, v38\n\t"
    "v_exp_f32 v39, v39\n\t"
    "v_exp_f32 v40, v40\n\t"
    "v_exp_f32 v41, v41\n\t"
    "v_smfmac_f32_32x32x32_bf16 a[16:31], a[56:59], v[32:39], v12\n\t"
    "v_exp_f32 v42, v42\n\t"
    "v_exp_f32 v43, v43\n\t"
    "v_exp_f32 v44, v44\n\t"
    "v_exp_f32 v45, v45\n\t"
    "v_exp_f32 v46, v46\n\t"
    "v_exp_f32 v47, v47\n\t"
    "v_exp_f32 v16, v16\n\t"
    "v_exp_f32 v17, v17\n\t"
    "v_smfmac_f32_32x32x32_bf16 a[16:31], a[60:63], v[40:47], v12\n\t"
    "v_exp_f32 v18, v18\n\t"
    "v_exp_f32 v19, v19\n\t"
    "v_exp_f32 v20, v20\n\t"
    "v_exp_f32 v21, v21\n\t"
    "v_mfma_f32_32x32x16_bf16 v[32:47], a[40:43], a[36:39], v[64:79]\n\t"
    "v_exp_f32 v22, v22\n\t"
    "v_exp_f32 v23, v23\n\t"
    "v_exp_f32 v24, v24\n\t"
    "ds_read_b128 a[44:47], v2 offset:46080\n\t"
    "v_exp_f32 v25, v25\n\t"
    "v_smfmac_f32_32x32x32_bf16 a[0:15], a[48:51], v[16:23], v12\n\t"
    "v_exp_f32 v26, v26\n\t"
    "v_exp_f32 v27, v27\n\t"
    "v_exp_f32 v28, v28\n\t"
    "s_mov_b64 exec, s[60:61]\n\t"
    "ds_read_b128 a[56:59], v3 offset:46080\n\t"
    "ds_read_b128 a[60:63], v3 offset:46592\n\t"
    "s_mov_b64 exec, -1\n\t"
    "v_exp_f32 v29, v29\n\t"
    "v_exp_f32 v30, v30\n\t"
    "v_exp_f32 v31, v31\n\t"
    "v_exp_f32 v32, v32\n\t"
    "ds_read_b32 v14, v4 offset:8\n\t"
    "v_exp_f32 v33, v33\n\t"
    "v_smfmac_f32_32x32x32_bf16 a[0:15], a[52:55], v[24:31], v12\n\t"
    "v_exp_f32 v34, v34\n\t"
    "v_exp_f32 v35, v35\n\t"
    "v_exp_f32 v36, v36\n\t"
    "s_waitcnt lgkmcnt(0)\n\t"
    "v_mfma_f32_32x32x16_bf16 v[16:31], a[44:47], a[32:35], v[48:63]\n\t"
    "v_exp_f32 v37, v37\n\t"
    "v_exp_f32 v38, v38\n\t"
    "v_exp_f32 v39, v39\n\t"
    "v_exp_f32 v40, v40\n\t"
    "v_exp_f32 v41, v41\n\t"
    "v_smfmac_f32_32x32x32_bf16 a[16:31], a[48:51], v[32:39], v12\n\t"
    "v_exp_f32 v42, v42\n\t"
    "v_exp_f32 v43, v43\n\t"
    "v_exp_f32 v44, v44\n\t"
    "v_exp_f32 v45, v45\n\t"
    "v_exp_f32 v46, v46\n\t"
    "v_exp_f32 v47, v47\n\t"
    "v_readfirstlane_b32 s6, v14\n\t"
    "s_cmp_ge_u32 s6, 4\n\t"
    "s_cbranch_scc0 .LX32_%=\n\t"
    ".LX33_%=:\n\t"
    "v_exp_f32 v16, v16\n\t"
    "v_exp_f32 v17, v17\n\t"
    "v_smfmac_f32_32x32x32_bf16 a[16:31], a[52:55], v[40:47], v12\n\t"
    "v_exp_f32 v18, v18\n\t"
    "v_exp_f32 v19, v19\n\t"
    "v_exp_f32 v20, v20\n\t"
    "v_exp_f32 v21, v21\n\t"
    "v_mfma_f32_32x32x16_bf16 v[32:47], a[44:47], a[36:39], v[64:79]\n\t"
    "v_exp_f32 v22, v22\n\t"
    "v_exp_f32 v23, v23\n\t"
    "v_exp_f32 v24, v24\n\t"
    "ds_read_b128 a[40:43], v2 offset:0\n\t"
    "v_exp_f32 v25, v25\n\t"
    "v_smfmac_f32_32x32x32_bf16 a[0:15], a[56:59], v[16:23], v12\n\t"
    "v_exp_f32 v26, v26\n\t"
    "v_exp_f32 v27, v27\n\t"
    "v_exp_f32 v28, v28\n\t"
    "s_mov_b64 exec, s[60:61]\n\t"
    "ds_read_b128 a[48:51], v3 offset:0\n\t"
    "ds_read_b128 a[52:55], v3 offset:512\n\t"
    "s_mov_b64 exec, 1\n\t"
    "ds_add_u32 v4, v13 offset:36\n\t"
    "s_mov_b64 exec, -1\n\t"
    "v_exp_f32 v29, v29\n\t"
    "v_exp_f32 v30, v30\n\t"
    "v_exp_f32 v31, v31\n\t"
    "v_exp_f32 v32, v32\n\t"
    "v_exp_f32 v33, v33\n\t"
    "v_smfmac_f32_32x32x32_bf16 a[0:15], a[60:63], v[24:31], v12\n\t"
    "v_exp_f32 v34, v34\n\t"
    "v_exp_f32 v35, v35\n\t"
    "v_exp_f32 v36, v36\n\t"
    "s_waitcnt lgkmcnt(0)\n\t"
    "v_mfma_f32_32x32x16_bf16 v[16:31], a[40:43], a[32:35], v[48:63]\n\t"
    "v_exp_f32 v37, v37\n\t"
    "v_exp_f32 v38, v38\n\t"
    "v_exp_f32 v39, v39\n\t"
    "v_exp_f32 v40, v40\n\t"
    "v_exp_f32 v41, v41\n\t"
    "v_smfmac_f32_32x32x32_bf16 a[16:31], a[56:59], v[32:39], v12\n\t"
    "v_exp_f32 v42, v42\n\t"
    "v_exp_f32 v43, v43\n\t"
    "v_exp_f32 v44, v44\n\t"
    "v_exp_f32 v45, v45\n\t"
    "v_exp_f32 v46, v46\n\t"
    "v_exp_f32 v47, v47\n\t"
    "v_exp_f32 v16, v16\n\t"
    "v_exp_f32 v17, v17\n\t"
    "v_smfmac_f32_32x32x32_bf16 a[16:31], a[60:63], v[40:47], v12\n\t"
    "v_exp_f32 v18, v18\n\t"
    "v_exp_f32 v19, v19\n\t"
    "v_exp_f32 v20, v20\n\t"
    "v_exp_f32 v21, v21\n\t"
    "v_mfma_f32_32x32x16_bf16 v[32:47], a[40:43], a[36:39], v[64:79]\n\t"
    "v_exp_f32 v22, v22\n\t"
    "v_exp_f32 v23, v23\n\t"
    "v_exp_f32 v24, v24\n\t"
    "ds_read_b128 a[44:47], v2 offset:1024\n\t"
    "v_exp_f32 v25, v25\n\t"
    "v_smfmac_f32_32x32x32_bf16 a[0:15], a[48:51], v[16:23], v12\n\t"
    "v_exp_f32 v26, v26\n\t"
    "v_exp_f32 v27, v27\n\t"
    "v_exp_f32 v28, v28\n\t"
    "s_mov_b64 exec, s[60:61]\n\t"
    "ds_read_b128 a[56:59], v3 offset:1024\n\t"
    "ds_read_b128 a[60:63], v3 offset:1536\n\t"
    "s_mov_b64 exec, -1\n\t"
    "v_exp_f32 v29, v29\n\t"
    "v_exp_f32 v30, v30\n\t"
    "v_exp_f32 v31, v31\n\t"
    "v_exp_f32 v32, v32\n\t"
    "v_exp_f32 v33, v33\n\t"
    "v_smfmac_f32_32x32x32_bf16 a[0:15], a[52:55], v[24:31], v12\n\t"
    "v_exp_f32 v34, v34\n\t"
    "v_exp_f32 v35, v35\n\t"
    "v_exp_f32 v36, v36\n\t"
    "s_waitcnt lgkmcnt(0)\n\t"
    "v_mfma_f32_32x32x16_bf16 v[16:31], a[44:47], a[32:35], v[48:63]\n\t"
    "v_exp_f32 v37, v37\n\t"
    "v_exp_f32 v38, v38\n\t"
    "v_exp_f32 v39, v39\n\t"
    "v_exp_f32 v40, v40\n\t"
    "v_exp_f32 v41, v41\n\t"
    "v_smfmac_f32_32x32x32_bf16 a[16:31], a[48:51], v[32:39], v12\n\t"
    "v_exp_f32 v42, v42\n\t"
    "v_exp_f32 v43, v43\n\t"
    "v_exp_f32 v44, v44\n\t"
    "v_exp_f32 v45, v45\n\t"
    "v_exp_f32 v46, v46\n\t"
    "v_exp_f32 v47, v47\n\t"
    "v_exp_f32 v16, v16\n\t"
    "v_exp_f32 v17, v17\n\t"
    "v_smfmac_f32_32x32x32_bf16 a[16:31], a[52:55], v[40:47], v12\n\t"
    "v_exp_f32 v18, v18\n\t"
    "v_exp_f32 v19, v19\n\t"
    "v_exp_f32 v20, v20\n\t"
    "v_exp_f32 v21, v21\n\t"
    "v_mfma_f32_32x32x16_bf16 v[32:47], a[44:47], a[36:39], v[64:79]\n\t"
    "v_exp_f32 v22, v22\n\t"
    "v_exp_f32 v23, v23\n\t"
    "v_exp_f32 v24, v24\n\t"
    "ds_read_b128 a[40:43], v2 offset:4096\n\t"
    "v_exp_f32 v25, v25\n\t"
    "v_smfmac_f32_32x32x32_bf16 a[0:15], a[56:59], v[16:23], v12\n\t"
    "v_exp_f32 v26, v26\n\t"
    "v_exp_f32 v27, v27\n\t"
    "v_exp_f32 v28, v28\n\t"
    "s_mov_b64 exec, s[60:61]\n\t"
    "ds_read_b128 a[48:51], v3 offset:4096\n\t"
    "ds_read_b128 a[52:55], v3 offset:4608\n\t"
    "s_mov_b64 exec, -1\n\t"
    "v_exp_f32 v29, v29\n\t"
    "v_exp_f32 v30, v30\n\t"
    "v_exp_f32 v31, v31\n\t"
    "v_exp_f32 v32, v32\n\t"
    "v_exp_f32 v33, v33\n\t"
    "v_smfmac_f32_32x32x32_bf16 a[0:15], a[60:63], v[24:31], v12\n\t"
    "v_exp_f32 v34, v34\n\t"
    "v_exp_f32 v35, v35\n\t"
    "v_exp_f32 v36, v36\n\t"
    "s_waitcnt lgkmcnt(0)\n\t"
    "v_mfma_f32_32x32x16_bf16 v[16:31], a[40:43], a[32:35], v[48:63]\n\t"
    "v_exp_f32 v37, v37\n\t"
    "v_exp_f32 v38, v38\n\t"
    "v_exp_f32 v39, v39\n\t"
    "v_exp_f32 v40, v40\n\t"
    "v_exp_f32 v41, v41\n\t"
    "v_smfmac_f32_32x32x32_bf16 a[16:31], a[56:59], v[32:39], v12\n\t"
    "v_exp_f32 v42, v42\n\t"
    "v_exp_f32 v43, v43\n\t"
    "v_exp_f32 v44, v44\n\t"
    "v_exp_f32 v45, v45\n\t"
    "v_exp_f32 v46, v46\n\t"
    "v_exp_f32 v47, v47\n\t"
    "v_exp_f32 v16, v16\n\t"
    "v_exp_f32 v17, v17\n\t"
    "v_smfmac_f32_32x32x32_bf16 a[16:31], a[60:63], v[40:47], v12\n\t"
    "v_exp_f32 v18, v18\n\t"
    "v_exp_f32 v19, v19\n\t"
    "v_exp_f32 v20, v20\n\t"
    "v_exp_f32 v21, v21\n\t"
    "v_mfma_f32_32x32x16_bf16 v[32:47], a[40:43], a[36:39], v[64:79]\n\t"
    "v_exp_f32 v22, v22\n\t"
    "v_exp_f32 v23, v23\n\t"
    "v_exp_f32 v24, v24\n\t"
    "ds_read_b128 a[44:47], v2 offset:5120\n\t"
    "v_exp_f32 v25, v25\n\t"
    "v_smfmac_f32_32x32x32_bf16 a[0:15], a[48:51], v[16:23], v12\n\t"
    "v_exp_f32 v26, v26\n\t"
    "v_exp_f32 v27, v27\n\t"
    "v_exp_f32 v28, v28\n\t"
    "s_mov_b64 exec, s[60:61]\n\t"
    "ds_read_b128 a[56:59], v3 offset:5120\n\t"
    "ds_read_b128 a[60:63], v3 offset:5632\n\t"
    "s_mov_b64 exec, -1\n\t"
    "v_exp_f32 v29, v29\n\t"
    "v_exp_f32 v30, v30\n\t"
    "v_exp_f32 v31, v31\n\t"
    "v_exp_f32 v32, v32\n\t"
    "v_exp_f32 v33, v33\n\t"
    "v_smfmac_f32_32x32x32_bf16 a[0:15], a[52:55], v[24:31], v12\n\t"
    "v_exp_f32 v34, v34\n\t"
    "v_exp_f32 v35, v35\n\t"
    "v_exp_f32 v36, v36\n\t"
    "s_waitcnt lgkmcnt(0)\n\t"
    "v_mfma_f32_32x32x16_bf16 v[16:31], a[44:47], a[32:35], v[48:63]\n\t"
    "v_exp_f32 v37, v37\n\t"
    "v_exp_f32 v38, v38\n\t"
    "v_exp_f32 v39, v39\n\t"
    "v_exp_f32 v40, v40\n\t"
    "v_exp_f32 v41, v41\n\t"
    "v_smfmac_f32_32x32x32_bf16 a[16:31], a[48:51], v[32:39], v12\n\t"
    "v_exp_f32 v42, v42\n\t"
    "v_exp_f32 v43, v43\n\t"
    "v_exp_f32 v44, v44\n\t"
    "v_exp_f32 v45, v45\n\t"
    "v_exp_f32 v46, v46\n\t"
    "v_exp_f32 v47, v47\n\t"
    "v_exp_f32 v16, v16\n\t"
    "v_exp_f32 v17, v17\n\t"
    "v_smfmac_f32_32x32x32_bf16 a[16:31], a[52:55], v[40:47], v12\n\t"
    "v_exp_f32 v18, v18\n\t"
    "v_exp_f32 v19, v19\n\t"
    "v_exp_f32 v20, v20\n\t"
    "v_exp_f32 v21, v21\n\t"
    "v_mfma_f32_32x32x16_bf16 v[32:47], a[44:47], a[36:39], v[64:79]\n\t"
    "v_exp_f32 v22, v22\n\t"
    "v_exp_f32 v23, v23\n\t"
    "v_exp_f32 v24, v24\n\t"
    "ds_read_b128 a[40:43], v2 offset:8192\n\t"
    "v_exp_f32 v25, v25\n\t"
    "v_smfmac_f32_32x32x32_bf16 a[0:15], a[56:59], v[16:23], v12\n\t"
    "v_exp_f32 v26, v26\n\t"
    "v_exp_f32 v27, v27\n\t"
    "v_exp_f32 v28, v28\n\t"
    "s_mov_b64 exec, s[60:61]\n\t"
    "ds_read_b128 a[48:51], v3 offset:8192\n\t"
    "ds_read_b128 a[52:55], v3 offset:8704\n\t"
    "s_mov_b64 exec, -1\n\t"
    "v_exp_f32 v29, v29\n\t"
    "v_exp_f32 v30, v30\n\t"
    "v_exp_f32 v31, v31\n\t"
    "v_exp_f32 v32, v32\n\t"
    "v_exp_f32 v33, v33\n\t"
    "v_smfmac_f32_32x32x32_bf16 a[0:15], a[60:63], v[24:31], v12\n\t"
    "v_exp_f32 v34, v34\n\t"
    "v_exp_f32 v35, v35\n\t"
    "v_exp_f32 v36, v36\n\t"
    "s_waitcnt lgkmcnt(0)\n\t"
    "v_mfma_f32_32x32x16_bf16 v[16:31], a[40:43], a[32:35], v[48:63]\n\t"
    "v_exp_f32 v37, v37\n\t"
    "v_exp_f32 v38, v38\n\t"
    "v_exp_f32 v39, v39\n\t"
    "v_exp_f32 v40, v40\n\t"
    "v_exp_f32 v41, v41\n\t"
    "v_smfmac_f32_32x32x32_bf16 a[16:31], a[56:59], v[32:39], v12\n\t"
    "v_exp_f32 v42, v42\n\t"
    "v_exp_f32 v43, v43\n\t"
    "v_exp_f32 v44, v44\n\t"
    "v_exp_f32 v45, v45\n\t"
    "v_exp_f32 v46, v46\n\t"
    "v_exp_f32 v47, v47\n\t"
    "v_exp_f32 v16, v16\n\t"
    "v_exp_f32 v17, v17\n\t"
    "v_smfmac_f32_32x32x32_bf16 a[16:31], a[60:63], v[40:47], v12\n\t"
    "v_exp_f32 v18, v18\n\t"
    "v_exp_f32 v19, v19\n\t"
    "v_exp_f32 v20, v20\n\t"
    "v_exp_f32 v21, v21\n\t"
    "v_mfma_f32_32x32x16_bf16 v[32:47], a[40:43], a[36:39], v[64:79]\n\t"
    "v_exp_f32 v22, v22\n\t"
    "v_exp_f32 v23, v23\n\t"
    "v_exp_f32 v24, v24\n\t"
    "ds_read_b128 a[44:47], v2 offset:9216\n\t"
    "v_exp_f32 v25, v25\n\t"
    "v_smfmac_f32_32x32x32_bf16 a[0:15], a[48:51], v[16:23], v12\n\t"
    "v_exp_f32 v26, v26\n\t"
    "v_exp_f32 v27, v27\n\t"
    "v_exp_f32 v28, v28\n\t"
    "s_mov_b64 exec, s[60:61]\n\t"
    "ds_read_b128 a[56:59], v3 offset:9216\n\t"
    "ds_read_b128 a[60:63], v3 offset:9728\n\t"
    "s_mov_b64 exec, -1\n\t"
    "v_exp_f32 v29, v29\n\t"
    "v_exp_f32 v30, v30\n\t"
    "v_exp_f32 v31, v31\n\t"
    "v_exp_f32 v32, v32\n\t"
    "v_exp_f32 v33, v33\n\t"
    "v_smfmac_f32_32x32x32_bf16 a[0:15], a[52:55], v[24:31], v12\n\t"
    "v_exp_f32 v34, v34\n\t"
    "v_exp_f32 v35, v35\n\t"
    "v_exp_f32 v36, v36\n\t"
    "s_waitcnt lgkmcnt(0)\n\t"
    "v_mfma_f32_32x32x16_bf16 v[16:31], a[44:47], a[32:35], v[48:63]\n\t"
    "v_exp_f32 v37, v37\n\t"
    "v_exp_f32 v38, v38\n\t"
    "v_exp_f32 v39, v39\n\t"
    "v_exp_f32 v40, v40\n\t"
    "v_exp_f32 v41, v41\n\t"
    "v_smfmac_f32_32x32x32_bf16 a[16:31], a[48:51], v[32:39], v12\n\t"
    "v_exp_f32 v42, v42\n\t"
    "v_exp_f32 v43, v43\n\t"
    "v_exp_f32 v44, v44\n\t"
    "v_exp_f32 v45, v45\n\t"
    "v_exp_f32 v46, v46\n\t"
    "v_exp_f32 v47, v47\n\t"
    "v_exp_f32 v16, v16\n\t"
    "v_exp_f32 v17, v17\n\t"
    "v_smfmac_f32_32x32x32_bf16 a[16:31], a[52:55], v[40:47], v12\n\t"
    "v_exp_f32 v18, v18\n\t"
    "v_exp_f32 v19, v19\n\t"
    "v_exp_f32 v20, v20\n\t"
    "v_exp_f32 v21, v21\n\t"
    "v_mfma_f32_32x32x16_bf16 v[32:47], a[44:47], a[36:39], v[64:79]\n\t"
    "v_exp_f32 v22, v22\n\t"
    "v_exp_f32 v23, v23\n\t"
    "v_exp_f32 v24, v24\n\t"
    "ds_read_b128 a[40:43], v2 offset:12288\n\t"
    "v_exp_f32 v25, v25\n\t"
    "v_smfmac_f32_32x32x32_bf16 a[0:15], a[56:59], v[16:23], v12\n\t"
    "v_exp_f32 v26, v26\n\t"
    "v_exp_f32 v27, v27\n\t"
    "v_exp_f32 v28, v28\n\t"
    "s_mov_b64 exec, s[60:61]\n\t"
    "ds_read_b128 a[48:51], v3 offset:12288\n\t"
    "ds_read_b128 a[52:55], v3 offset:12800\n\t"
    "s_mov_b64 exec, -1\n\t"
    "v_exp_f32 v29, v29\n\t"
    "v_exp_f32 v30, v30\n\t"
    "v_exp_f32 v31, v31\n\t"
    "v_exp_f32 v32, v32\n\t"
    "v_exp_f32 v33, v33\n\t"
    "v_smfmac_f32_32x32x32_bf16 a[0:15], a[60:63], v[24:31], v12\n\t"
    "v_exp_f32 v34, v34\n\t"
    "v_exp_f32 v35, v35\n\t"
    "v_exp_f32 v36, v36\n\t"
    "s_waitcnt lgkmcnt(0)\n\t"
    "v_mfma_f32_32x32x16_bf16 v[16:31], a[40:43], a[32:35], v[48:63]\n\t"
    "v_exp_f32 v37, v37\n\t"
    "v_exp_f32 v38, v38\n\t"
    "v_exp_f32 v39, v39\n\t"
    "v_exp_f32 v40, v40\n\t"
    "v_exp_f32 v41, v41\n\t"
    "v_smfmac_f32_32x32x32_bf16 a[16:31], a[56:59], v[32:39], v12\n\t"
    "v_exp_f32 v42, v42\n\t"
    "v_exp_f32 v43, v43\n\t"
    "v_exp_f32 v44, v44\n\t"
    "v_exp_f32 v45, v45\n\t"
    "v_exp_f32 v46, v46\n\t"
    "v_exp_f32 v47, v47\n\t"
    "v_exp_f32 v16, v16\n\t"
    "v_exp_f32 v17, v17\n\t"
    "v_smfmac_f32_32x32x32_bf16 a[16:31], a[60:63], v[40:47], v12\n\t"
    "v_exp_f32 v18, v18\n\t"
    "v_exp_f32 v19, v19\n\t"
    "v_exp_f32 v20, v20\n\t"
    "v_exp_f32 v21, v21\n\t"
    "v_mfma_f32_32x32x16_bf16 v[32:47], a[40:43], a[36:39], v[64:79]\n\t"
    "v_exp_f32 v22, v22\n\t"
    "v_exp_f32 v23, v23\n\t"
    "v_exp_f32 v24, v24\n\t"
    "ds_read_b128 a[44:47], v2 offset:13312\n\t"
    "v_exp_f32 v25, v25\n\t"
    "v_smfmac_f32_32x32x32_bf16 a[0:15], a[48:51], v[16:23], v12\n\t"
    "v_exp_f32 v26, v26\n\t"
    "v_exp_f32 v27, v27\n\t"
    "v_exp_f32 v28, v28\n\t"
    "s_mov_b64 exec, s[60:61]\n\t"
    "ds_read_b128 a[56:59], v3 offset:13312\n\t"
    "ds_read_b128 a[60:63], v3 offset:13824\n\t"
    "s_mov_b64 exec, -1\n\t"
    "v_exp_f32 v29, v29\n\t"
    "v_exp_f32 v30, v30\n\t"
    "v_exp_f32 v31, v31\n\t"
    "v_exp_f32 v32, v32\n\t"
    "v_exp_f32 v33, v33\n\t"
    "v_smfmac_f32_32x32x32_bf16 a[0:15], a[52:55], v[24:31], v12\n\t"
    "v_exp_f32 v34, v34\n\t"
    "v_exp_f32 v35, v35\n\t"
    "v_exp_f32 v36, v36\n\t"
    "s_waitcnt lgkmcnt(0)\n\t"
    "v_mfma_f32_32x32x16_bf16 v[16:31], a[44:47], a[32:35], v[48:63]\n\t"
    "v_exp_f32 v37, v37\n\t"
    "v_exp_f32 v38, v38\n\t"
    "v_exp_f32 v39, v39\n\t"
    "v_exp_f32 v40, v40\n\t"
    "v_exp_f32 v41, v41\n\t"
    "v_smfmac_f32_32x32x32_bf16 a[16:31], a[48:51], v[32:39], v12\n\t"
    "v_exp_f32 v42, v42\n\t"
    "v_exp_f32 v43, v43\n\t"
    "v_exp_f32 v44, v44\n\t"
    "v_exp_f32 v45, v45\n\t"
    "v_exp_f32 v46, v46\n\t"
    "v_exp_f32 v47, v47\n\t"
    "v_add_u32 v2, 0x20000, v2\n\t"
    "v_add_u32 v3, 0x20000, v3\n\t"
    "v_exp_f32 v16, v16\n\t"
    "v_exp_f32 v17, v17\n\t"
    "v_smfmac_f32_32x32x32_bf16 a[16:31], a[52:55], v[40:47], v12\n\t"
    "v_exp_f32 v18, v18\n\t"
    "v_exp_f32 v19, v19\n\t"
    "v_exp_f32 v20, v20\n\t"
    "v_exp_f32 v21, v21\n\t"
    "v_mfma_f32_32x32x16_bf16 v[32:47], a[44:47], a[36:39], v[64:79]\n\t"
    "v_exp_f32 v22, v22\n\t"
    "v_exp_f32 v23, v23\n\t"
    "v_exp_f32 v24, v24\n\t"
    "ds_read_b128 a[40:43], v2 offset:0\n\t"
    "v_exp_f32 v25, v25\n\t"
    "v_smfmac_f32_32x32x32_bf16 a[0:15], a[56:59], v[16:23], v12\n\t"
    "v_exp_f32 v26, v26\n\t"
    "v_exp_f32 v27, v27\n\t"
    "v_exp_f32 v28, v28\n\t"
    "s_mov_b64 exec, s[60:61]\n\t"
    "ds_read_b128 a[48:51], v3 offset:0\n\t"
    "ds_read_b128 a[52:55], v3 offset:512\n\t"
    "s_mov_b64 exec, 1\n\t"
    "ds_add_u32 v4, v13 offset:40\n\t"
    "s_mov_b64 exec, -1\n\t"
    "v_exp_f32 v29, v29\n\t"
    "v_exp_f32 v30, v30\n\t"
    "v_exp_f32 v31, v31\n\t"
    "v_exp_f32 v32, v32\n\t"
    "v_exp_f32 v33, v33\n\t"
    "v_smfmac_f32_32x32x32_bf16 a[0:15], a[60:63], v[24:31], v12\n\t"
    "v_exp_f32 v34, v34\n\t"
    "v_exp_f32 v35, v35\n\t"
    "v_exp_f32 v36, v36\n\t"
    "s_waitcnt lgkmcnt(0)\n\t"
    "v_mfma_f32_32x32x16_bf16 v[16:31], a[40:43], a[32:35], v[48:63]\n\t"
    "v_exp_f32 v37, v37\n\t"
    "v_exp_f32 v38, v38\n\t"
    "v_exp_f32 v39, v39\n\t"
    "v_exp_f32 v40, v40\n\t"
    "v_exp_f32 v41, v41\n\t"
    "v_smfmac_f32_32x32x32_bf16 a[16:31], a[56:59], v[32:39], v12\n\t"
    "v_exp_f32 v42, v42\n\t"
    "v_exp_f32 v43, v43\n\t"
    "v_exp_f32 v44, v44\n\t"
    "v_exp_f32 v45, v45\n\t"
    "v_exp_f32 v46, v46\n\t"
    "v_exp_f32 v47, v47\n\t"
    "v_exp_f32 v16, v16\n\t"
    "v_exp_f32 v17, v17\n\t"
    "v_smfmac_f32_32x32x32_bf16 a[16:31], a[60:63], v[40:47], v12\n\t"
    "v_exp_f32 v18, v18\n\t"
    "v_exp_f32 v19, v19\n\t"
    "v_exp_f32 v20, v20\n\t"
    "v_exp_f32 v21, v21\n\t"
    "v_mfma_f32_32x32x16_bf16 v[32:47], a[40:43], a[36:39], v[64:79]\n\t"
    "v_exp_f32 v22, v22\n\t"
    "v_exp_f32 v23, v23\n\t"
    "v_exp_f32 v24, v24\n\t"
    "ds_read_b128 a[44:47], v2 offset:1024\n\t"
    "v_exp_f32 v25, v25\n\t"
    "v_smfmac_f32_32x32x32_bf16 a[0:15], a[48:51], v[16:23], v12\n\t"
    "v_exp_f32 v26, v26\n\t"
    "v_exp_f32 v27, v27\n\t"
    "v_exp_f32 v28, v28\n\t"
    "s_mov_b64 exec, s[60:61]\n\t"
    "ds_read_b128 a[56:59], v3 offset:1024\n\t"
    "ds_read_b128 a[60:63], v3 offset:1536\n\t"
    "s_mov_b64 exec, -1\n\t"
    "v_exp_f32 v29, v29\n\t"
    "v_exp_f32 v30, v30\n\t"
    "v_exp_f32 v31, v31\n\t"
    "v_exp_f32 v32, v32\n\t"
    "v_exp_f32 v33, v33\n\t"
    "v_smfmac_f32_32x32x32_bf16 a[0:15], a[52:55], v[24:31], v12\n\t"
    "v_exp_f32 v34, v34\n\t"
    "v_exp_f32 v35, v35\n\t"
    "v_exp_f32 v36, v36\n\t"
    "s_waitcnt lgkmcnt(0)\n\t"
    "v_mfma_f32_32x32x16_bf16 v[16:31], a[44:47], a[32:35], v[48:63]\n\t"
    "v_exp_f32 v37, v37\n\t"
    "v_exp_f32 v38, v38\n\t"
    "v_exp_f32 v39, v39\n\t"
    "v_exp_f32 v40, v40\n\t"
    "v_exp_f32 v41, v41\n\t"
    "v_smfmac_f32_32x32x32_bf16 a[16:31], a[48:51], v[32:39], v12\n\t"
    "v_exp_f32 v42, v42\n\t"
    "v_exp_f32 v43, v43\n\t"
    "v_exp_f32 v44, v44\n\t"
    "v_exp_f32 v45, v45\n\t"
    "v_exp_f32 v46, v46\n\t"
    "v_exp_f32 v47, v47\n\t"
    "v_exp_f32 v16, v16\n\t"
    "v_exp_f32 v17, v17\n\t"
    "v_smfmac_f32_32x32x32_bf16 a[16:31], a[52:55], v[40:47], v12\n\t"
    "v_exp_f32 v18, v18\n\t"
    "v_exp_f32 v19, v19\n\t"
    "v_exp_f32 v20, v20\n\t"
    "v_exp_f32 v21, v21\n\t"
    "v_mfma_f32_32x32x16_bf16 v[32:47], a[44:47], a[36:39], v[64:79]\n\t"
    "v_exp_f32 v22, v22\n\t"
    "v_exp_f32 v23, v23\n\t"
    "v_exp_f32 v24, v24\n\t"
    "ds_read_b128 a[40:43], v2 offset:4096\n\t"
    "v_exp_f32 v25, v25\n\t"
    "v_smfmac_f32_32x32x32_bf16 a[0:15], a[56:59], v[16:23], v12\n\t"
    "v_exp_f32 v26, v26\n\t"
    "v_exp_f32 v27, v27\n\t"
    "v_exp_f32 v28, v28\n\t"
    "s_mov_b64 exec, s[60:61]\n\t"
    "ds_read_b128 a[48:51], v3 offset:4096\n\t"
    "ds_read_b128 a[52:55], v3 offset:4608\n\t"
    "s_mov_b64 exec, -1\n\t"
    "v_exp_f32 v29, v29\n\t"
    "v_exp_f32 v30, v30\n\t"
    "v_exp_f32 v31, v31\n\t"
    "v_exp_f32 v32, v32\n\t"
    "v_exp_f32 v33, v33\n\t"
    "v_smfmac_f32_32x32x32_bf16 a[0:15], a[60:63], v[24:31], v12\n\t"
    "v_exp_f32 v34, v34\n\t"
    "v_exp_f32 v35, v35\n\t"
    "v_exp_f32 v36, v36\n\t"
    "s_waitcnt lgkmcnt(0)\n\t"
    "v_mfma_f32_32x32x16_bf16 v[16:31], a[40:43], a[32:35], v[48:63]\n\t"
    "v_exp_f32 v37, v37\n\t"
    "v_exp_f32 v38, v38\n\t"
    "v_exp_f32 v39, v39\n\t"
    "v_exp_f32 v40, v40\n\t"
    "v_exp_f32 v41, v41\n\t"
    "v_smfmac_f32_32x32x32_bf16 a[16:31], a[56:59], v[32:39], v12\n\t"
    "v_exp_f32 v42, v42\n\t"
    "v_exp_f32 v43, v43\n\t"
    "v_exp_f32 v44, v44\n\t"
    "v_exp_f32 v45, v45\n\t"
    "v_exp_f32 v46, v46\n\t"
    "v_exp_f32 v47, v47\n\t"
    "s_setprio 1\n\t"
    "v_exp_f32 v16, v16\n\t"
    "v_exp_f32 v17, v17\n\t"
    "v_smfmac_f32_32x32x32_bf16 a[16:31], a[60:63], v[40:47], v12\n\t"
    "v_exp_f32 v18, v18\n\t"
    "v_exp_f32 v19, v19\n\t"
    "v_exp_f32 v20, v20\n\t"
    "v_exp_f32 v21, v21\n\t"
    "v_mfma_f32_32x32x16_bf16 v[32:47], a[40:43], a[36:39], v[64:79]\n\t"
    "v_exp_f32 v22, v22\n\t"
    "v_exp_f32 v23, v23\n\t"
    "v_exp_f32 v24, v24\n\t"
    "ds_read_b128 a[44:47], v2 offset:5120\n\t"
    "v_exp_f32 v25, v25\n\t"
    "v_smfmac_f32_32x32x32_bf16 a[0:15], a[48:51], v[16:23], v12\n\t"
    "v_exp_f32 v26, v26\n\t"
    "v_exp_f32 v27, v27\n\t"
    "v_exp_f32 v28, v28\n\t"
    "s_mov_b64 exec, s[60:61]\n\t"
    "ds_read_b128 a[56:59], v3 offset:5120\n\t"
    "ds_read_b128 a[60:63], v3 offset:5632\n\t"
    "s_mov_b64 exec, -1\n\t"
    "v_exp_f32 v29, v29\n\t"
    "v_exp_f32 v30, v30\n\t"
    "v_exp_f32 v31, v31\n\t"
    "v_exp_f32 v32, v32\n\t"
    "v_exp_f32 v33, v33\n\t"
    "v_smfmac_f32_32x32x32_bf16 a[0:15], a[52:55], v[24:31], v12\n\t"
    "v_exp_f32 v34, v34\n\t"
    "v_exp_f32 v35, v35\n\t"
    "v_exp_f32 v36, v36\n\t"
    "s_waitcnt lgkmcnt(0)\n\t"
    "v_mfma_f32_32x32x16_bf16 v[16:31], a[44:47], a[32:35], v[48:63]\n\t"
    "v_exp_f32 v37, v37\n\t"
    "v_exp_f32 v38, v38\n\t"
    "v_exp_f32 v39, v39\n\t"
    "v_exp_f32 v40, v40\n\t"
    "v_exp_f32 v41, v41\n\t"
    "v_smfmac_f32_32x32x32_bf16 a[16:31], a[48:51], v[32:39], v12\n\t"
    "v_exp_f32 v42, v42\n\t"
    "v_exp_f32 v43, v43\n\t"
    "v_exp_f32 v44, v44\n\t"
    "v_exp_f32 v45, v45\n\t"
    "v_exp_f32 v46, v46\n\t"
    "v_exp_f32 v47, v47\n\t"
    "v_exp_f32 v16, v16\n\t"
    "v_exp_f32 v17, v17\n\t"
    "v_smfmac_f32_32x32x32_bf16 a[16:31], a[52:55], v[40:47], v12\n\t"
    "v_exp_f32 v18, v18\n\t"
    "v_exp_f32 v19, v19\n\t"
    "v_exp_f32 v20, v20\n\t"
    "v_exp_f32 v21, v21\n\t"
    "v_mfma_f32_32x32x16_bf16 v[32:47], a[44:47], a[36:39], v[64:79]\n\t"
    "v_exp_f32 v22, v22\n\t"
    "v_exp_f32 v23, v23\n\t"
    "v_exp_f32 v24, v24\n\t"
    "ds_read_b128 a[40:43], v2 offset:8192\n\t"
    "v_exp_f32 v25, v25\n\t"
    "v_smfmac_f32_32x32x32_bf16 a[0:15], a[56:59], v[16:23], v12\n\t"
    "v_exp_f32 v26, v26\n\t"
    "v_exp_f32 v27, v27\n\t"
    "v_exp_f32 v28, v28\n\t"
    "s_mov_b64 exec, s[60:61]\n\t"
    "ds_read_b128 a[48:51], v3 offset:8192\n\t"
    "ds_read_b128 a[52:55], v3 offset:8704\n\t"
    "s_mov_b64 exec, -1\n\t"
    "v_exp_f32 v29, v29\n\t"
    "v_exp_f32 v30, v30\n\t"
    "v_exp_f32 v31, v31\n\t"
    "v_exp_f32 v32, v32\n\t"
    "v_exp_f32 v33, v33\n\t"
    "v_smfmac_f32_32x32x32_bf16 a[0:15], a[60:63], v[24:31], v12\n\t"
    "v_exp_f32 v34, v34\n\t"
    "v_exp_f32 v35, v35\n\t"
    "v_exp_f32 v36, v36\n\t"
    "s_waitcnt lgkmcnt(0)\n\t"
    "v_mfma_f32_32x32x16_bf16 v[16:31], a[40:43], a[32:35], v[48:63]\n\t"
    "v_exp_f32 v37, v37\n\t"
    "v_exp_f32 v38, v38\n\t"
    "v_exp_f32 v39, v39\n\t"
    "v_exp_f32 v40, v40\n\t"
    "v_exp_f32 v41, v41\n\t"
    "v_smfmac_f32_32x32x32_bf16 a[16:31], a[56:59], v[32:39], v12\n\t"
    "v_exp_f32 v42, v42\n\t"
    "v_exp_f32 v43, v43\n\t"
    "v_exp_f32 v44, v44\n\t"
    "v_exp_f32 v45, v45\n\t"
    "v_exp_f32 v46, v46\n\t"
    "v_exp_f32 v47, v47\n\t"
    "v_exp_f32 v16, v16\n\t"
    "v_exp_f32 v17, v17\n\t"
    "v_smfmac_f32_32x32x32_bf16 a[16:31], a[60:63], v[40:47], v12\n\t"
    "v_exp_f32 v18, v18\n\t"
    "v_exp_f32 v19, v19\n\t"
    "v_exp_f32 v20, v20\n\t"
    "v_exp_f32 v21, v21\n\t"
    "v_mfma_f32_32x32x16_bf16 v[32:47], a[40:43], a[36:39], v[64:79]\n\t"
    "v_exp_f32 v22, v22\n\t"
    "v_exp_f32 v23, v23\n\t"
    "v_exp_f32 v24, v24\n\t"
    "ds_read_b128 a[44:47], v2 offset:9216\n\t"
    "v_exp_f32 v25, v25\n\t"
    "v_smfmac_f32_32x32x32_bf16 a[0:15], a[48:51], v[16:23], v12\n\t"
    "v_exp_f32 v26, v26\n\t"
    "v_exp_f32 v27, v27\n\t"
    "v_exp_f32 v28, v28\n\t"
    "s_mov_b64 exec, s[60:61]\n\t"
    "ds_read_b128 a[56:59], v3 offset:9216\n\t"
    "ds_read_b128 a[60:63], v3 offset:9728\n\t"
    "s_mov_b64 exec, -1\n\t"
    "v_exp_f32 v29, v29\n\t"
    "v_exp_f32 v30, v30\n\t"
    "v_exp_f32 v31, v31\n\t"
    "v_exp_f32 v32, v32\n\t"
    "v_exp_f32 v33, v33\n\t"
    "v_smfmac_f32_32x32x32_bf16 a[0:15], a[52:55], v[24:31], v12\n\t"
    "v_exp_f32 v34, v34\n\t"
    "v_exp_f32 v35, v35\n\t"
    "v_exp_f32 v36, v36\n\t"
    "s_waitcnt lgkmcnt(0)\n\t"
    "v_mfma_f32_32x32x16_bf16 v[16:31], a[44:47], a[32:35], v[48:63]\n\t"
    "v_exp_f32 v37, v37\n\t"
    "v_exp_f32 v38, v38\n\t"
    "v_exp_f32 v39, v39\n\t"
    "v_exp_f32 v40, v40\n\t"
    "v_exp_f32 v41, v41\n\t"
    "v_smfmac_f32_32x32x32_bf16 a[16:31], a[48:51], v[32:39], v12\n\t"
    "v_exp_f32 v42, v42\n\t"
    "v_exp_f32 v43, v43\n\t"
    "v_exp_f32 v44, v44\n\t"
    "v_exp_f32 v45, v45\n\t"
    "v_exp_f32 v46, v46\n\t"
    "v_exp_f32 v47, v47\n\t"
    "v_exp_f32 v16, v16\n\t"
    "v_exp_f32 v17, v17\n\t"
    "v_smfmac_f32_32x32x32_bf16 a[16:31], a[52:55], v[40:47], v12\n\t"
    "v_exp_f32 v18, v18\n\t"
    "v_exp_f32 v19, v19\n\t"
    "v_exp_f32 v20, v20\n\t"
    "v_exp_f32 v21, v21\n\t"
    "v_mfma_f32_32x32x16_bf16 v[32:47], a[44:47], a[36:39], v[64:79]\n\t"
    "v_exp_f32 v22, v22\n\t"
    "v_exp_f32 v23, v23\n\t"
    "v_exp_f32 v24, v24\n\t"
    "ds_read_b128 a[40:43], v2 offset:12288\n\t"
    "v_exp_f32 v25, v25\n\t"
    "v_smfmac_f32_32x32x32_bf16 a[0:15], a[56:59], v[16:23], v12\n\t"
    "v_exp_f32 v26, v26\n\t"
    "v_exp_f32 v27, v27\n\t"
    "v_exp_f32 v28, v28\n\t"
    "s_mov_b64 exec, s[60:61]\n\t"
    "ds_read_b128 a[48:51], v3 offset:12288\n\t"
    "ds_read_b128 a[52:55], v3 offset:12800\n\t"
    "s_mov_b64 exec, -1\n\t"
    "v_exp_f32 v29, v29\n\t"
    "v_exp_f32 v30, v30\n\t"
    "v_exp_f32 v31, v31\n\t"
    "v_exp_f32 v32, v32\n\t"
    "v_exp_f32 v33, v33\n\t"
    "v_smfmac_f32_32x32x32_bf16 a[0:15], a[60:63], v[24:31], v12\n\t"
    "v_exp_f32 v34, v34\n\t"
    "v_exp_f32 v35, v35\n\t"
    "v_exp_f32 v36, v36\n\t"
    "s_waitcnt lgkmcnt(0)\n\t"
    "v_mfma_f32_32x32x16_bf16 v[16:31], a[40:43], a[32:35], v[48:63]\n\t"
    "v_exp_f32 v37, v37\n\t"
    "v_exp_f32 v38, v38\n\t"
    "v_exp_f32 v39, v39\n\t"
    "v_exp_f32 v40, v40\n\t"
    "v_exp_f32 v41, v41\n\t"
    "v_smfmac_f32_32x32x32_bf16 a[16:31], a[56:59], v[32:39], v12\n\t"
    "v_exp_f32 v42, v42\n\t"
    "v_exp_f32 v43, v43\n\t"
    "v_exp_f32 v44, v44\n\t"
    "v_exp_f32 v45, v45\n\t"
    "v_exp_f32 v46, v46\n\t"
    "v_exp_f32 v47, v47\n\t"
    "v_exp_f32 v16, v16\n\t"
    "v_exp_f32 v17, v17\n\t"
    "v_smfmac_f32_32x32x32_bf16 a[16:31], a[60:63], v[40:47], v12\n\t"
    "v_exp_f32 v18, v18\n\t"
    "v_exp_f32 v19, v19\n\t"
    "v_exp_f32 v20, v20\n\t"
    "v_exp_f32 v21, v21\n\t"
    "v_mfma_f32_32x32x16_bf16 v[32:47], a[40:43], a[36:39], v[64:79]\n\t"
    "v_exp_f32 v22, v22\n\t"
    "v_exp_f32 v23, v23\n\t"
    "v_exp_f32 v24, v24\n\t"
    "ds_read_b128 a[44:47], v2 offset:13312\n\t"
    "v_exp_f32 v25, v25\n\t"
    "v_smfmac_f32_32x32x32_bf16 a[0:15], a[48:51], v[16:23], v12\n\t"
    "v_exp_f32 v26, v26\n\t"
    "v_exp_f32 v27, v27\n\t"
    "v_exp_f32 v28, v28\n\t"
    "s_mov_b64 exec, s[60:61]\n\t"
    "ds_read_b128 a[56:59], v3 offset:13312\n\t"
    "ds_read_b128 a[60:63], v3 offset:13824\n\t"
    "s_mov_b64 exec, -1\n\t"
    "v_exp_f32 v29, v29\n\t"
    "v_exp_f32 v30, v30\n\t"
    "v_exp_f32 v31, v31\n\t"
    "v_exp_f32 v32, v32\n\t"
    "v_exp_f32 v33, v33\n\t"
    "v_smfmac_f32_32x32x32_bf16 a[0:15], a[52:55], v[24:31], v12\n\t"
    "v_exp_f32 v34, v34\n\t"
    "v_exp_f32 v35, v35\n\t"
    "v_exp_f32 v36, v36\n\t"
    "s_waitcnt lgkmcnt(0)\n\t"
    "v_mfma_f32_32x32x16_bf16 v[16:31], a[44:47], a[32:35], v[48:63]\n\t"
    "v_exp_f32 v37, v37\n\t"
    "v_exp_f32 v38, v38\n\t"
    "v_exp_f32 v39, v39\n\t"
    "v_exp_f32 v40, v40\n\t"
    "v_exp_f32 v41, v41\n\t"
    "v_smfmac_f32_32x32x32_bf16 a[16:31], a[48:51], v[32:39], v12\n\t"
    "v_exp_f32 v42, v42\n\t"
    "v_exp_f32 v43, v43\n\t"
    "v_exp_f32 v44, v44\n\t"
    "v_exp_f32 v45, v45\n\t"
    "v_exp_f32 v46, v46\n\t"
    "v_exp_f32 v47, v47\n\t"
    "v_exp_f32 v16, v16\n\t"
    "v_exp_f32 v17, v17\n\t"
    "v_smfmac_f32_32x32x32_bf16 a[16:31], a[52:55], v[40:47], v12\n\t"
    "v_exp_f32 v18, v18\n\t"
    "v_exp_f32 v19, v19\n\t"
    "v_exp_f32 v20, v20\n\t"
    "v_exp_f32 v21, v21\n\t"
    "v_mfma_f32_32x32x16_bf16 v[32:47], a[44:47], a[36:39], v[64:79]\n\t"
    "v_exp_f32 v22, v22\n\t"
    "v_exp_f32 v23, v23\n\t"
    "v_exp_f32 v24, v24\n\t"
    "v_exp_f32 v25, v25\n\t"
    "v_smfmac_f32_32x32x32_bf16 a[0:15], a[56:59], v[16:23], v12\n\t"
    "v_exp_f32 v26, v26\n\t"
    "v_exp_f32 v27, v27\n\t"
    "v_exp_f32 v28, v28\n\t"
    "s_mov_b64 exec, 1\n\t"
    "ds_add_u32 v4, v13 offset:44\n\t"
    "s_mov_b64 exec, -1\n\t"
    "v_exp_f32 v29, v29\n\t"
    "v_exp_f32 v30, v30\n\t"
    "v_exp_f32 v31, v31\n\t"
    "v_exp_f32 v32, v32\n\t"
    "v_exp_f32 v33, v33\n\t"
    "v_smfmac_f32_32x32x32_bf16 a[0:15], a[60:63], v[24:31], v12\n\t"
    "v_exp_f32 v34, v34\n\t"
    "v_exp_f32 v35, v35\n\t"
    "v_exp_f32 v36, v36\n\t"
    "v_exp_f32 v37, v37\n\t"
    "v_exp_f32 v38, v38\n\t"
    "v_exp_f32 v39, v39\n\t"
    "ds_read_b32 v14, v6 offset:160\n\t"
    "v_exp_f32 v40, v40\n\t"
    "v_exp_f32 v41, v41\n\t"
    "v_smfmac_f32_32x32x32_bf16 a[16:31], a[56:59], v[32:39], v12\n\t"
    "v_exp_f32 v42, v42\n\t"
    "v_exp_f32 v43, v43\n\t"
    "v_exp_f32 v44, v44\n\t"
    "v_exp_f32 v45, v45\n\t"
    "v_exp_f32 v46, v46\n\t"
    "v_exp_f32 v47, v47\n\t"
    ".LXp175_%=:\n\t"
    "s_cbranch_execz .LXp183_%=\n\t"
    "s_nop 0\n\t"
    "v_smfmac_f32_32x32x32_bf16 a[16:31], a[60:63], v[40:47], v12\n\t"
    "s_waitcnt lgkmcnt(0)\n\t"
    "v_readfirstlane_b32 s10, v14\n\t"
    "s_cmp_eq_u32 s10, 0\n\t"
    "s_cbranch_scc1 .LX38_%=\n\t"
    ".LX36_%=:\n\t"
    "ds_read_b128 v[48:51], v11 offset:2048\n\t"
    "ds_read_b128 v[52:55], v11 offset:3072\n\t"
    "ds_read_b32 v56, v9 offset:128\n\t"
    "ds_read_b128 v[64:67], v11\n\t"
    "ds_read_b128 v[68:71], v11 offset:1024\n\t"
    "ds_read_b32 v72, v9\n\t"
    "ds_read_b64 v[82:83], v10 offset:192\n\t"
    "ds_read_b32 v80, v10 offset:156\n\t"
    "v_accvgpr_read_b32 v16, a0\n\t"
    "v_accvgpr_read_b32 v17, a1\n\t"
    "v_accvgpr_read_b32 v18, a2\n\t"
    "v_accvgpr_read_b32 v19, a3\n\t"
    "v_accvgpr_read_b32 v20, a4\n\t"
    "v_accvgpr_read_b32 v21, a5\n\t"
    "v_accvgpr_read_b32 v22, a6\n\t"
    "v_accvgpr_read_b32 v23, a7\n\t"
    "v_accvgpr_read_b32 v24, a8\n\t"
    "v_accvgpr_read_b32 v32, a16\n\t"
    "v_accvgpr_read_b32 v33, a17\n\t"
    "v_accvgpr_read_b32 v34, a18\n\t"
    "v_accvgpr_read_b32 v35, a19\n\t"
    "v_accvgpr_read_b32 v36, a20\n\t"
    "v_accvgpr_read_b32 v37, a21\n\t"
    "v_accvgpr_read_b32 v38, a22\n\t"
    "v_accvgpr_read_b32 v39, a23\n\t"
    "v_accvgpr_read_b32 v40, a24\n\t"
    "s_waitcnt lgkmcnt(0)\n\t"
    "v_add_f32 v24, v24, v56\n\t"
    "v_add_f32 v40, v40, v72\n\t"
    "v_readfirstlane_b32 s11, v82\n\t"
    "v_pk_add_f32 v[16:17], v[16:17], v[48:49]\n\t"
    "v_pk_add_f32 v[18:19], v[18:19], v[50:51]\n\t"
    "v_pk_add_f32 v[20:21], v[20:21], v[52:53]\n\t"
    "v_pk_add_f32 v[22:23], v[22:23], v[54:55]\n\t"
    "v_pk_add_f32 v[32:33], v[32:33], v[64:65]\n\t"
    "v_pk_add_f32 v[34:35], v[34:35], v[66:67]\n\t"
    "v_pk_add_f32 v[36:37], v[36:37], v[68:69]\n\t"
    "v_pk_add_f32 v[38:39], v[38:39], v[70:71]\n\t"
    ".LXfbr7_%=:\n\t"
    "v_rcp_f32 v26, v24\n\t"
    "v_rcp_f32 v42, v40\n\t"
    "v_maximum3_f32 v44, |v24|, |v40|, |v16|\n\t"
    "v_maximum3_f32 v44, |v17|, |v18|, v44\n\t"
    "v_maximum3_f32 v44, |v19|, |v20|, v44\n\t"
    "v_maximum3_f32 v44, |v21|, |v22|, v44\n\t"
    "v_maximum3_f32 v44, |v23|, |v32|, v44\n\t"
    "v_maximum3_f32 v44, |v33|, |v34|, v44\n\t"
    "v_maximum3_f32 v44, |v35|, |v36|, v44\n\t"
    "v_maximum3_f32 v44, |v37|, |v38|, v44\n\t"
    "v_maximum3_f32 v44, |v39|, |v39|, v44\n\t"
    "v_pk_mul_f32 v[16:17], v[16:17], v[26:27] op_sel_hi:[1,0]\n\t"
    "v_pk_mul_f32 v[18:19], v[18:19], v[26:27] op_sel_hi:[1,0]\n\t"
    "v_pk_mul_f32 v[20:21], v[20:21], v[26:27] op_sel_hi:[1,0]\n\t"
    "v_pk_mul_f32 v[22:23], v[22:23], v[26:27] op_sel_hi:[1,0]\n\t"
    "v_pk_mul_f32 v[32:33], v[32:33], v[42:43] op_sel_hi:[1,0]\n\t"
    "v_pk_mul_f32 v[34:35], v[34:35], v[42:43] op_sel_hi:[1,0]\n\t"
    "v_pk_mul_f32 v[36:37], v[36:37], v[42:43] op_sel_hi:[1,0]\n\t"
    "v_pk_mul_f32 v[38:39], v[38:39], v[42:43] op_sel_hi:[1,0]\n\t"
    "v_cvt_pk_bf16_f32 v16, v16, v17\n\t"
    ".LXp176_%=:\n\t"
    "s_cbranch_execz .LXp184_%=\n\t"
    "v_cvt_pk_bf16_f32 v17, v18, v19\n\t"
    "v_cvt_pk_bf16_f32 v18, v20, v21\n\t"
    "v_cvt_pk_bf16_f32 v19, v22, v23\n\t"
    "v_cvt_pk_bf16_f32 v20, v32, v33\n\t"
    "v_cvt_pk_bf16_f32 v21, v34, v35\n\t"
    "v_cvt_pk_bf16_f32 v22, v36, v37\n\t"
    "v_cvt_pk_bf16_f32 v23, v38, v39\n\t"
    "s_mov_b32 s6, 1568669696\n\t"
    "s_cmp_ge_u32 s11, 4\n\t"
    "s_cbranch_scc0 .LX41_%=\n\t"
    ".LX42_%=:\n\t"
    "v_cmp_nge_f32 vcc, s6, v44\n\t"
    "s_cbranch_vccnz .LXfb6_%=\n\t"
    ".LX40_%=:\n\t"
    "buffer_store_dwordx4 v[16:19], v81, s[36:39], s74 offen sc1\n\t"
    "buffer_store_dwordx4 v[20:23], v81, s[36:39], s75 offen sc1\n\t"
    "s_mov_b64 exec, 1\n\t"
    "ds_add_rtn_u32 v9, v10, v13 offset:152\n\t"
    "ds_add_u32 v6, v12 offset:160\n\t"
    "s_mov_b64 exec, -1\n\t"
    ".LX37_%=:\n\t"
    "v_mov_b32 v12, v7\n\t"
    "v_mov_b32 v2, v5\n\t"
    "v_readfirstlane_b32 s6, v82\n\t"
    "v_readfirstlane_b32 s7, v83\n\t"
    "s_cmp_eq_u32 s59, 0\n\t"
    "s_cselect_b32 s6, s6, s7\n\t"
    "s_cmp_ge_u32 s6, 4\n\t"
    "s_cbranch_scc0 .LX43_%=\n\t"
    ".LX44_%=:\n\t"
    "v_xor_b32 v3, 32, v2\n\t"
    "v_xor_b32 v4, 64, v2\n\t"
    "v_xor_b32 v5, 0x60, v2\n\t"
    "ds_read_b64_tr_b16 a[0:1], v2 offset:0\n\t"
    "ds_read_b64_tr_b16 a[2:3], v2 offset:1024\n\t"
    "ds_read_b64_tr_b16 a[4:5], v3 offset:0\n\t"
    "ds_read_b64_tr_b16 a[6:7], v3 offset:1024\n\t"
    "ds_read_b64_tr_b16 a[8:9], v4 offset:0\n\t"
    "ds_read_b64_tr_b16 a[10:11], v4 offset:1024\n\t"
    "ds_read_b64_tr_b16 a[12:13], v5 offset:0\n\t"
    "ds_read_b64_tr_b16 a[14:15], v5 offset:1024\n\t"
    "ds_read_b64_tr_b16 a[16:17], v2 offset:4096\n\t"
    "ds_read_b64_tr_b16 a[18:19], v2 offset:5120\n\t"
    "ds_read_b64_tr_b16 a[20:21], v3 offset:4096\n\t"
    "ds_read_b64_tr_b16 a[22:23], v3 offset:5120\n\t"
    "ds_read_b64_tr_b16 a[24:25], v4 offset:4096\n\t"
    "ds_read_b64_tr_b16 a[26:27], v4 offset:5120\n\t"
    "ds_read_b64_tr_b16 a[28:29], v5 offset:4096\n\t"
    "ds_read_b64_tr_b16 a[30:31], v5 offset:5120\n\t"
    "ds_read_b64_tr_b16 a[32:33], v2 offset:8192\n\t"
    "ds_read_b64_tr_b16 a[34:35], v2 offset:9216\n\t"
    "ds_read_b64_tr_b16 a[36:37], v3 offset:8192\n\t"
    "ds_read_b64_tr_b16 a[38:39], v3 offset:9216\n\t"
    "ds_read_b64_tr_b16 a[40:41], v4 offset:8192\n\t"
    "ds_read_b64_tr_b16 a[42:43], v4 offset:9216\n\t"
    "ds_read_b64_tr_b16 a[44:45], v5 offset:8192\n\t"
    "ds_read_b64_tr_b16 a[46:47], v5 offset:9216\n\t"
    "s_waitcnt lgkmcnt(15)\n\t"
    "v_readfirstlane_b32 s10, v9\n\t"
    "s_cmp_eq_u32 s10, 3\n\t"
    "s_cbranch_scc1 .LX45_%=\n\t"
    ".LX47_%=:\n\t"
    "ds_read_b64_tr_b16 a[48:49], v2 offset:12288\n\t"
    "ds_read_b64_tr_b16 a[50:51], v2 offset:13312\n\t"
    "ds_read_b64_tr_b16 a[52:53], v3 offset:12288\n\t"
    "ds_read_b64_tr_b16 a[54:55], v3 offset:13312\n\t"
    "ds_read_b64_tr_b16 a[56:57], v4 offset:12288\n\t"
    "ds_read_b64_tr_b16 a[58:59], v4 offset:13312\n\t"
    "ds_read_b64_tr_b16 a[60:61], v5 offset:12288\n\t"
    "ds_read_b64_tr_b16 a[62:63], v5 offset:13312\n\t"
    "ds_read_b64_tr_b16 a[64:65], v2 offset:16384\n\t"
    "ds_read_b64_tr_b16 a[66:67], v2 offset:17408\n\t"
    ".LXp177_%=:\n\t"
    "s_cbranch_execz .LXp185_%=\n\t"
    "ds_read_b64_tr_b16 a[68:69], v3 offset:16384\n\t"
    "ds_read_b64_tr_b16 a[70:71], v3 offset:17408\n\t"
    "ds_read_b64_tr_b16 a[72:73], v4 offset:16384\n\t"
    "ds_read_b64_tr_b16 a[74:75], v4 offset:17408\n\t"
    "ds_read_b64_tr_b16 a[76:77], v5 offset:16384\n\t"
    "ds_read_b64_tr_b16 a[78:79], v5 offset:17408\n\t"
    "ds_read_b64_tr_b16 v[64:65], v2 offset:20480\n\t"
    "ds_read_b64_tr_b16 v[66:67], v2 offset:21504\n\t"
    "ds_read_b64_tr_b16 v[68:69], v3 offset:20480\n\t"
    "ds_read_b64_tr_b16 v[70:71], v3 offset:21504\n\t"
    "ds_read_b64_tr_b16 v[72:73], v4 offset:20480\n\t"
    "ds_read_b64_tr_b16 v[74:75], v4 offset:21504\n\t"
    "ds_read_b64_tr_b16 v[76:77], v5 offset:20480\n\t"
    "ds_read_b64_tr_b16 v[78:79], v5 offset:21504\n\t"
    "s_cmp_eq_u32 s10, 3\n\t"
    "s_cbranch_scc1 .LX48_%=\n\t"
    "s_mov_b32 s7, 4194304\n\t"
    ".LXw49_%=:\n\t"
    "ds_read_b32 v14, v10 offset:152\n\t"
    "s_waitcnt lgkmcnt(0)\n\t"
    "v_readfirstlane_b32 s6, v14\n\t"
    "s_cmp_ge_u32 s6, 4\n\t"
    "s_cbranch_scc1 .LXd50_%=\n\t"
    "s_sleep 1\n\t"
    "s_sub_u32 s7, s7, 1\n\t"
    "s_cmp_lg_u32 s7, 0\n\t"
    "s_cbranch_scc1 .LXw49_%=\n\t"
    ".LXd50_%=:\n\t"
    "s_setprio 1\n\t"
    "s_sleep 2\n\t"
    ".LX45_%=:\n\t"
    "v_readfirstlane_b32 s6, v80\n\t"
    "s_add_u32 s6, s6, 0\n\t"
    ".LX51_%=:\n\t"
    "s_cmp_lt_i32 s6, 2\n\t"
    "s_cbranch_scc1 .LX52_%=\n\t"
    "s_sleep 1\n\t"
    "s_sub_u32 s6, s6, 4\n\t"
    "s_branch .LX51_%=\n\t"
    ".LX52_%=:\n\t"
    "buffer_load_dwordx4 v[16:19], v1, s[36:39], s64 offen offset:0 sc1\n\t"
    "buffer_load_dwordx4 v[20:23], v1, s[36:39], s64 offen offset:1024 sc1\n\t"
    "buffer_load_dwordx4 v[24:27], v1, s[36:39], s64 offen offset:2048 sc1\n\t"
    "buffer_load_dwordx4 v[28:31], v1, s[36:39], s64 offen offset:3072 sc1\n\t"
    "s_sleep 2\n\t"
    "buffer_load_dwordx4 v[32:35], v1, s[36:39], s65 offen offset:0 sc1\n\t"
    "buffer_load_dwordx4 v[36:39], v1, s[36:39], s65 offen offset:1024 sc1\n\t"
    "buffer_load_dwordx4 v[40:43], v1, s[36:39], s65 offen offset:2048 sc1\n\t"
    "buffer_load_dwordx4 v[44:47], v1, s[36:39], s65 offen offset:3072 sc1\n\t"
    "s_mov_b64 exec, 1\n\t"
    "ds_add_u32 v10, v13 offset:200\n\t"
    "s_mov_b64 exec, -1\n\t"
    "s_cmp_eq_u32 s10, 3\n\t"
    "s_cbranch_scc1 .LX47_%=\n\t"
    ".LX48_%=:\n\t"
    "s_nop 0\n\t"
    "s_waitcnt vmcnt(7) lgkmcnt(0)\n\t"
    "v_mfma_f32_16x16x32_bf16 v[48:51], a[0:3], v[16:19], 0\n\t"
    "v_max3_u32 v14, v16, v17, v17\n\t"
    "v_mfma_f32_16x16x32_bf16 v[52:55], a[4:7], v[16:19], 0\n\t"
    "v_max3_u32 v14, v18, v19, v14\n\t"
    "v_mfma_f32_16x16x32_bf16 v[56:59], a[8:11], v[16:19], 0\n\t"
    "v_mfma_f32_16x16x32_bf16 v[60:63], a[12:15], v[16:19], 0\n\t"
    "s_waitcnt vmcnt(6)\n\t"
    "v_mfma_f32_16x16x32_bf16 v[48:51], a[16:19], v[20:23], v[48:51]\n\t"
    "v_max3_u32 v14, v20, v21, v14\n\t"
    "ds_read_b64_tr_b16 a[0:1], v2 offset:24576\n\t"
    "ds_read_b64_tr_b16 a[2:3], v2 offset:25600\n\t"
    "v_mfma_f32_16x16x32_bf16 v[52:55], a[20:23], v[20:23], v[52:55]\n\t"
    "v_max3_u32 v14, v22, v23, v14\n\t"
    "ds_read_b64_tr_b16 a[4:5], v3 offset:24576\n\t"
    "ds_read_b64_tr_b16 a[6:7], v3 offset:25600\n\t"
    "v_mfma_f32_16x16x32_bf16 v[56:59], a[24:27], v[20:23], v[56:59]\n\t"
    "ds_read_b64_tr_b16 a[8:9], v4 offset:24576\n\t"
    "ds_read_b64_tr_b16 a[10:11], v4 offset:25600\n\t"
    "v_mfma_f32_16x16x32_bf16 v[60:63], a[28:31], v[20:23], v[60:63]\n\t"
    "ds_read_b64_tr_b16 a[12:13], v5 offset:24576\n\t"
    "ds_read_b64_tr_b16 a[14:15], v5 offset:25600\n\t"
    "s_waitcnt vmcnt(5)\n\t"
    "v_mfma_f32_16x16x32_bf16 v[48:51], a[32:35], v[24:27], v[48:51]\n\t"
    ".LXp178_%=:\n\t"
    "s_cbranch_execz .LXp186_%=\n\t"
    "v_max3_u32 v14, v24, v25, v14\n\t"
    "ds_read_b64_tr_b16 a[16:17], v2 offset:28672\n\t"
    "ds_read_b64_tr_b16 a[18:19], v2 offset:29696\n\t"
    "v_mfma_f32_16x16x32_bf16 v[52:55], a[36:39], v[24:27], v[52:55]\n\t"
    "v_max3_u32 v14, v26, v27, v14\n\t"
    "ds_read_b64_tr_b16 a[20:21], v3 offset:28672\n\t"
    "ds_read_b64_tr_b16 a[22:23], v3 offset:29696\n\t"
    "v_mfma_f32_16x16x32_bf16 v[56:59], a[40:43], v[24:27], v[56:59]\n\t"
    "ds_read_b64_tr_b16 a[24:25], v4 offset:28672\n\t"
    "ds_read_b64_tr_b16 a[26:27], v4 offset:29696\n\t"
    "v_mfma_f32_16x16x32_bf16 v[60:63], a[44:47], v[24:27], v[60:63]\n\t"
    "ds_read_b64_tr_b16 a[28:29], v5 offset:28672\n\t"
    "ds_read_b64_tr_b16 a[30:31], v5 offset:29696\n\t"
    "s_waitcnt vmcnt(4)\n\t"
    "v_mfma_f32_16x16x32_bf16 v[48:51], a[48:51], v[28:31], v[48:51]\n\t"
    "v_max3_u32 v14, v28, v29, v14\n\t"
    "v_mfma_f32_16x16x32_bf16 v[52:55], a[52:55], v[28:31], v[52:55]\n\t"
    "v_max3_u32 v14, v30, v31, v14\n\t"
    "v_mfma_f32_16x16x32_bf16 v[56:59], a[56:59], v[28:31], v[56:59]\n\t"
    "v_mfma_f32_16x16x32_bf16 v[60:63], a[60:63], v[28:31], v[60:63]\n\t"
    "s_waitcnt vmcnt(3)\n\t"
    "v_mfma_f32_16x16x32_bf16 v[48:51], a[64:67], v[32:35], v[48:51]\n\t"
    "v_max3_u32 v14, v32, v33, v14\n\t"
    "v_mfma_f32_16x16x32_bf16 v[52:55], a[68:71], v[32:35], v[52:55]\n\t"
    "v_max3_u32 v14, v34, v35, v14\n\t"
    "v_mfma_f32_16x16x32_bf16 v[56:59], a[72:75], v[32:35], v[56:59]\n\t"
    "v_mfma_f32_16x16x32_bf16 v[60:63], a[76:79], v[32:35], v[60:63]\n\t"
    "s_waitcnt vmcnt(2)\n\t"
    "v_mfma_f32_16x16x32_bf16 v[48:51], v[64:67], v[36:39], v[48:51]\n\t"
    "v_max3_u32 v14, v36, v37, v14\n\t"
    "v_mfma_f32_16x16x32_bf16 v[52:55], v[68:71], v[36:39], v[52:55]\n\t"
    "v_max3_u32 v14, v38, v39, v14\n\t"
    "v_mfma_f32_16x16x32_bf16 v[56:59], v[72:75], v[36:39], v[56:59]\n\t"
    "v_mfma_f32_16x16x32_bf16 v[60:63], v[76:79], v[36:39], v[60:63]\n\t"
    "s_waitcnt vmcnt(1) lgkmcnt(0)\n\t"
    "v_mfma_f32_16x16x32_bf16 v[48:51], a[0:3], v[40:43], v[48:51]\n\t"
    "v_max3_u32 v14, v40, v41, v14\n\t"
    "v_mfma_f32_16x16x32_bf16 v[52:55], a[4:7], v[40:43], v[52:55]\n\t"
    "v_max3_u32 v14, v42, v43, v14\n\t"
    "v_mfma_f32_16x16x32_bf16 v[56:59], a[8:11], v[40:43], v[56:59]\n\t"
    "v_mfma_f32_16x16x32_bf16 v[60:63], a[12:15], v[40:43], v[60:63]\n\t"
    "s_waitcnt vmcnt(0)\n\t"
    "ds_read_b32 v7, v6 offset:176\n\t"
    "ds_read_b128 v[64:67], v11 offset:0\n\t"
    "ds_read_b128 v[68:71], v11 offset:1024\n\t"
    "ds_read_b128 v[72:75], v11 offset:2048\n\t"
    "ds_read_b128 v[76:79], v11 offset:3072\n\t"
    "v_mfma_f32_16x16x32_bf16 v[48:51], a[16:19], v[44:47], v[48:51]\n\t"
    "v_max3_u32 v14, v44, v45, v14\n\t"
    "v_mfma_f32_16x16x32_bf16 v[52:55], a[20:23], v[44:47], v[52:55]\n\t"
    "v_max3_u32 v14, v46, v47, v14\n\t"
    "v_mfma_f32_16x16x32_bf16 v[56:59], a[24:27], v[44:47], v[56:59]\n\t"
    "v_cmp_eq_u32 vcc, -1, v14\n\t"
    "v_mfma_f32_16x16x32_bf16 v[60:63], a[28:31], v[44:47], v[60:63]\n\t"
    "s_cbranch_vccnz .LX85_%=\n\t"
    ".LX86_%=:\n\t"
    "s_waitcnt lgkmcnt(0)\n\t"
    "v_readfirstlane_b32 s6, v7\n\t"
    "s_cmp_lg_u32 s6, 0\n\t"
    "s_cbranch_scc1 .LX87_%=\n\t"
    "s_nop 2\n\t"
    "ds_write_b128 v8, v[48:51] offset:0\n\t"
    "ds_write_b128 v8, v[52:55] offset:1024\n\t"
    "ds_write_b128 v8, v[56:59] offset:2048\n\t"
    "ds_write_b128 v8, v[60:63] offset:3072\n\t"
    "s_mov_b64 exec, 1\n\t"
    "ds_add_rtn_u32 v7, v6, v13 offset:176\n\t"
    "s_waitcnt lgkmcnt(0)\n\t"
    ".LXp179_%=:\n\t"
    "s_cbranch_execz .LXp187_%=\n\t"
    "v_readfirstlane_b32 s6, v7\n\t"
    "s_mov_b64 exec, -1\n\t"
    "s_cmp_eq_u32 s6, 0\n\t"
    "s_cbranch_scc1 .LXend5_%=\n\t"
    "ds_read_b128 v[64:67], v11 offset:0\n\t"
    "ds_read_b128 v[68:71], v11 offset:1024\n\t"
    "ds_read_b128 v[72:75], v11 offset:2048\n\t"
    "ds_read_b128 v[76:79], v11 offset:3072\n\t"
    "s_waitcnt lgkmcnt(0)\n\t"
    ".LX87_%=:\n\t"
    "s_nop 2\n\t"
    "v_pk_add_f32 v[48:49], v[48:49], v[64:65]\n\t"
    "v_pk_add_f32 v[50:51], v[50:51], v[66:67]\n\t"
    "v_pk_add_f32 v[52:53], v[52:53], v[68:69]\n\t"
    "v_pk_add_f32 v[54:55], v[54:55], v[70:71]\n\t"
    "v_cvt_pk_bf16_f32 v48, v48, v49\n\t"
    "v_cvt_pk_bf16_f32 v49, v50, v51\n\t"
    "v_cvt_pk_bf16_f32 v50, v52, v53\n\t"
    "v_cvt_pk_bf16_f32 v51, v54, v55\n\t"
    "s_nop 0\n\t"
    "v_permlane16_swap_b32 v48, v50\n\t"
    "v_permlane16_swap_b32 v49, v51\n\t"
    "global_store_dwordx4 v12, v[48:51], s[32:33] offset:1024 nt\n\t"
    "v_pk_add_f32 v[56:57], v[56:57], v[72:73]\n\t"
    "v_pk_add_f32 v[58:59], v[58:59], v[74:75]\n\t"
    "v_pk_add_f32 v[60:61], v[60:61], v[76:77]\n\t"
    "v_pk_add_f32 v[62:63], v[62:63], v[78:79]\n\t"
    "v_cvt_pk_bf16_f32 v56, v56, v57\n\t"
    "v_cvt_pk_bf16_f32 v57, v58, v59\n\t"
    "v_cvt_pk_bf16_f32 v58, v60, v61\n\t"
    "v_cvt_pk_bf16_f32 v59, v62, v63\n\t"
    "s_nop 0\n\t"
    "v_permlane16_swap_b32 v56, v58\n\t"
    "v_permlane16_swap_b32 v57, v59\n\t"
    "global_store_dwordx4 v12, v[56:59], s[32:33] offset:1088 nt\n\t"
    "s_branch .LXend5_%=\n\t"
    ".LX85_%=:\n\t"
    "s_sub_u32 s67, s67, 1\n\t"
    "s_cmp_le_i32 s67, 0\n\t"
    "s_cbranch_scc1 .LX86_%=\n\t"
    ".LX88_%=:\n\t"
    "v_max3_u32 v14, v16, v17, v18\n\t"
    "v_max_u32 v14, v14, v19\n\t"
    "v_cmp_eq_u32 vcc, -1, v14\n\t"
    "s_cbranch_vccz .LX89_%=\n\t"
    "buffer_load_dwordx4 v[16:19], v1, s[36:39], s64 offen offset:0 sc1\n\t"
    ".LX89_%=:\n\t"
    "v_max3_u32 v14, v20, v21, v22\n\t"
    "v_max_u32 v14, v14, v23\n\t"
    "v_cmp_eq_u32 vcc, -1, v14\n\t"
    "s_cbranch_vccz .LX90_%=\n\t"
    "buffer_load_dwordx4 v[20:23], v1, s[36:39], s64 offen offset:1024 sc1\n\t"
    ".LX90_%=:\n\t"
    "v_max3_u32 v14, v24, v25, v26\n\t"
    "v_max_u32 v14, v14, v27\n\t"
    "v_cmp_eq_u32 vcc, -1, v14\n\t"
    "s_cbranch_vccz .LX91_%=\n\t"
    "buffer_load_dwordx4 v[24:27], v1, s[36:39], s64 offen offset:2048 sc1\n\t"
    ".LX91_%=:\n\t"
    "v_max3_u32 v14, v28, v29, v30\n\t"
    "v_max_u32 v14, v14, v31\n\t"
    "v_cmp_eq_u32 vcc, -1, v14\n\t"
    "s_cbranch_vccz .LX92_%=\n\t"
    "buffer_load_dwordx4 v[28:31], v1, s[36:39], s64 offen offset:3072 sc1\n\t"
    ".LX92_%=:\n\t"
    "v_max3_u32 v14, v32, v33, v34\n\t"
    "v_max_u32 v14, v14, v35\n\t"
    "v_cmp_eq_u32 vcc, -1, v14\n\t"
    "s_cbranch_vccz .LX93_%=\n\t"
    "buffer_load_dwordx4 v[32:35], v1, s[36:39], s65 offen offset:0 sc1\n\t"
    ".LX93_%=:\n\t"
    "v_max3_u32 v14, v36, v37, v38\n\t"
    "v_max_u32 v14, v14, v39\n\t"
    "v_cmp_eq_u32 vcc, -1, v14\n\t"
    "s_cbranch_vccz .LX94_%=\n\t"
    "buffer_load_dwordx4 v[36:39], v1, s[36:39], s65 offen offset:1024 sc1\n\t"
    ".LX94_%=:\n\t"
    "v_max3_u32 v14, v40, v41, v42\n\t"
    "v_max_u32 v14, v14, v43\n\t"
    ".LXp180_%=:\n\t"
    "s_cbranch_execz .LXp188_%=\n\t"
    "v_cmp_eq_u32 vcc, -1, v14\n\t"
    "s_cbranch_vccz .LX95_%=\n\t"
    "buffer_load_dwordx4 v[40:43], v1, s[36:39], s65 offen offset:2048 sc1\n\t"
    ".LX95_%=:\n\t"
    "v_max3_u32 v14, v44, v45, v46\n\t"
    "v_max_u32 v14, v14, v47\n\t"
    "v_cmp_eq_u32 vcc, -1, v14\n\t"
    "s_cbranch_vccz .LX96_%=\n\t"
    "buffer_load_dwordx4 v[44:47], v1, s[36:39], s65 offen offset:3072 sc1\n\t"
    ".LX96_%=:\n\t"
    "ds_read_b64_tr_b16 a[0:1], v2 offset:0\n\t"
    "ds_read_b64_tr_b16 a[2:3], v2 offset:1024\n\t"
    "ds_read_b64_tr_b16 a[4:5], v3 offset:0\n\t"
    "ds_read_b64_tr_b16 a[6:7], v3 offset:1024\n\t"
    "ds_read_b64_tr_b16 a[8:9], v4 offset:0\n\t"
    "ds_read_b64_tr_b16 a[10:11], v4 offset:1024\n\t"
    "ds_read_b64_tr_b16 a[12:13], v5 offset:0\n\t"
    "ds_read_b64_tr_b16 a[14:15], v5 offset:1024\n\t"
    "ds_read_b64_tr_b16 a[16:17], v2 offset:4096\n\t"
    "ds_read_b64_tr_b16 a[18:19], v2 offset:5120\n\t"
    "ds_read_b64_tr_b16 a[20:21], v3 offset:4096\n\t"
    "ds_read_b64_tr_b16 a[22:23], v3 offset:5120\n\t"
    "ds_read_b64_tr_b16 a[24:25], v4 offset:4096\n\t"
    "ds_read_b64_tr_b16 a[26:27], v4 offset:5120\n\t"
    "ds_read_b64_tr_b16 a[28:29], v5 offset:4096\n\t"
    "ds_read_b64_tr_b16 a[30:31], v5 offset:5120\n\t"
    "ds_read_b64_tr_b16 v[64:65], v2 offset:20480\n\t"
    "ds_read_b64_tr_b16 v[66:67], v2 offset:21504\n\t"
    "ds_read_b64_tr_b16 v[68:69], v3 offset:20480\n\t"
    "ds_read_b64_tr_b16 v[70:71], v3 offset:21504\n\t"
    "ds_read_b64_tr_b16 v[72:73], v4 offset:20480\n\t"
    "ds_read_b64_tr_b16 v[74:75], v4 offset:21504\n\t"
    "ds_read_b64_tr_b16 v[76:77], v5 offset:20480\n\t"
    "ds_read_b64_tr_b16 v[78:79], v5 offset:21504\n\t"
    "s_waitcnt vmcnt(0)\n\t"
    "s_branch .LX48_%=\n\t"
    ".LX28_%=:\n\t"
    "s_mov_b32 s7, 4194304\n\t"
    ".LXw30_%=:\n\t"
    "ds_read_b32 v14, v4 offset:4\n\t"
    "s_waitcnt lgkmcnt(0)\n\t"
    "v_readfirstlane_b32 s6, v14\n\t"
    "s_cmp_ge_u32 s6, 4\n\t"
    "s_cbranch_scc1 .LXd31_%=\n\t"
    "s_sleep 2\n\t"
    "s_sub_u32 s7, s7, 1\n\t"
    "s_cmp_lg_u32 s7, 0\n\t"
    "s_cbranch_scc1 .LXw30_%=\n\t"
    ".LXd31_%=:\n\t"
    "s_branch .LX29_%=\n\t"
    ".LX32_%=:\n\t"
    "s_mov_b32 s7, 4194304\n\t"
    ".LXw34_%=:\n\t"
    "ds_read_b32 v14, v4 offset:8\n\t"
    "s_waitcnt lgkmcnt(0)\n\t"
    "v_readfirstlane_b32 s6, v14\n\t"
    "s_cmp_ge_u32 s6, 4\n\t"
    "s_cbranch_scc1 .LXd35_%=\n\t"
    "s_sleep 2\n\t"
    "s_sub_u32 s7, s7, 1\n\t"
    "s_cmp_lg_u32 s7, 0\n\t"
    "s_cbranch_scc1 .LXw34_%=\n\t"
    ".LXd35_%=:\n\t"
    "s_branch .LX33_%=\n\t"
    ".LX38_%=:\n\t"
    "v_xor_b32 v3, 0x400, v9\n\t"
    "ds_read_b64 v[82:83], v10 offset:192\n\t"
    "ds_read_b32 v80, v10 offset:156\n\t"
    "s_mov_b32 s7, 4194304\n\t"
    ".LXw97_%=:\n\t"
    "ds_read_b32 v14, v4 offset:44\n\t"
    "s_waitcnt lgkmcnt(0)\n\t"
    "v_readfirstlane_b32 s6, v14\n\t"
    "s_cmp_ge_u32 s6, 4\n\t"
    "s_cbranch_scc1 .LXd98_%=\n\t"
    "s_sleep 1\n\t"
    "s_sub_u32 s7, s7, 1\n\t"
    "s_cmp_lg_u32 s7, 0\n\t"
    "s_cbranch_scc1 .LXw97_%=\n\t"
    ".LXd98_%=:\n\t"
    "s_nop 1\n\t"
    "ds_write_b128 v8, a[0:3]\n\t"
    "ds_write_b128 v8, a[4:7] offset:1024\n\t"
    "ds_write_b32 v3, a8\n\t"
    "ds_write_b128 v8, a[16:19] offset:2048\n\t"
    "ds_write_b128 v8, a[20:23] offset:3072\n\t"
    ".LXp181_%=:\n\t"
    "s_cbranch_execz .LXp189_%=\n\t"
    "ds_write_b32 v3, a24 offset:128\n\t"
    "s_mov_b64 exec, 1\n\t"
    "ds_add_rtn_u32 v14, v6, v13 offset:160\n\t"
    "s_waitcnt lgkmcnt(0)\n\t"
    "v_readfirstlane_b32 s10, v14\n\t"
    "s_mov_b64 exec, -1\n\t"
    "s_cmp_lg_u32 s10, 0\n\t"
    "s_cbranch_scc1 .LX36_%=\n\t"
    "s_setprio 0\n\t"
    "s_branch .LX37_%=\n\t"
    ".LX41_%=:\n\t"
    "s_mov_b32 s7, 4194304\n\t"
    ".LXw99_%=:\n\t"
    "ds_read_b32 v14, v10 offset:192\n\t"
    "s_waitcnt lgkmcnt(0)\n\t"
    "v_readfirstlane_b32 s6, v14\n\t"
    "s_cmp_ge_u32 s6, 4\n\t"
    "s_cbranch_scc1 .LXd100_%=\n\t"
    "s_sleep 1\n\t"
    "s_sub_u32 s7, s7, 1\n\t"
    "s_cmp_lg_u32 s7, 0\n\t"
    "s_cbranch_scc1 .LXw99_%=\n\t"
    ".LXd100_%=:\n\t"
    "s_mov_b32 s6, 1568669696\n\t"
    "s_branch .LX42_%=\n\t"
    ".LX43_%=:\n\t"
    "v_lshl_add_u32 v3, s59, 2, v10\n\t"
    "s_mov_b32 s7, 4194304\n\t"
    ".LXw101_%=:\n\t"
    "ds_read_b32 v14, v3 offset:192\n\t"
    "s_waitcnt lgkmcnt(0)\n\t"
    "v_readfirstlane_b32 s6, v14\n\t"
    "s_cmp_ge_u32 s6, 4\n\t"
    "s_cbranch_scc1 .LXd102_%=\n\t"
    "s_sleep 2\n\t"
    "s_sub_u32 s7, s7, 1\n\t"
    "s_cmp_lg_u32 s7, 0\n\t"
    "s_cbranch_scc1 .LXw101_%=\n\t"
    ".LXd102_%=:\n\t"
    "s_branch .LX44_%=\n\t"
    ".LXG2_%=:\n\t"
    "s_barrier\n\t"
    "s_lshl_b32 s6, s2, 2\n\t"
    "s_and_b32 s4, s6, 24\n\t"
    "s_lshr_b32 s7, s2, 5\n\t"
    "s_add_u32 s4, s4, s7\n\t"
    "s_and_b32 s5, s6, 4\n\t"
    "s_bfe_u32 s7, s2, 0x20003\n\t"
    "s_or_b32 s5, s5, s7\n\t"
    "v_mov_b32 v13, 1\n\t"
    "v_and_b32 v6, 15, v0\n\t"
    "v_lshrrev_b32 v7, 1, v6\n\t"
    "v_xor_b32 v7, v7, v11\n\t"
    "v_lshlrev_b32 v6, 7, v6\n\t"
    "v_lshl_add_u32 v6, v7, 4, v6\n\t"
    "v_add_u32 v6, s9, v6\n\t"
    "v_xor_b32 v7, 64, v6\n\t"
    "v_bfe_u32 v8, v0, 2, 2\n\t"
    "v_lshl_add_u32 v9, v11, 3, v8\n\t"
    "v_lshlrev_b32 v9, 5, v9\n\t"
    "v_and_b32 v64, 3, v0\n\t"
    "v_bfe_u32 v65, v0, 1, 1\n\t"
    "v_and_b32 v66, 1, v0\n\t"
    "v_lshl_or_b32 v67, v66, 1, v65\n\t"
    "s_bitcmp1_b32 s78, 0\n\t"
    "s_cselect_b64 vcc, -1, 0\n\t"
    "v_cndmask_b32 v64, v67, v64, vcc\n\t"
    "v_lshl_add_u32 v9, v64, 3, v9\n\t"
    "v_add_u32 v9, s79, v9\n\t"
    "v_and_b32 v80, 31, v0\n\t"
    "v_lshlrev_b32 v80, 7, v80\n\t"
    "v_lshrrev_b32 v64, 5, v0\n\t"
    "v_bfe_u32 v67, v0, 1, 3\n\t"
    "v_xor_b32 v67, v67, v64\n\t"
    "v_lshl_add_u32 v80, v67, 4, v80\n\t"
    "s_and_b32 s59, s58, 1\n\t"
    "s_lshr_b32 s62, s58, 1\n\t"
    "s_lshl_b32 s6, s59, 12\n\t"
    "v_add_u32 v80, s6, v80\n\t"
    "v_lshl_add_u32 v81, v64, 3, v8\n\t"
    "v_lshlrev_b32 v81, 7, v81\n\t"
    "v_and_b32 v67, 1, v11\n\t"
    "v_lshl_or_b32 v67, v67, 1, v65\n\t"
    "v_lshrrev_b32 v8, 1, v8\n\t"
    "v_xor_b32 v8, s62, v8\n\t"
    "v_lshl_or_b32 v67, v8, 2, v67\n\t"
    ".LXp182_%=:\n\t"
    "s_cbranch_execz .LXp190_%=\n\t"
    "v_lshl_add_u32 v81, v66, 3, v81\n\t"
    "v_lshl_add_u32 v81, v67, 4, v81\n\t"
    "s_lshl_b32 s6, s59, 5\n\t"
    "v_xor_b32 v79, s6, v81\n\t"
    "v_and_b32 v67, 1, v11\n\t"
    "v_lshl_add_u32 v79, v67, 13, v79\n\t"
    "v_mov_b32 v16, 897988541\n\t"
    "v_mov_b32 v17, v16\n\t"
    "v_mov_b64 v[18:19], v[16:17]\n\t"
    "v_mov_b64 v[20:21], v[16:17]\n\t"
    "v_mov_b64 v[22:23], v[16:17]\n\t"
    "v_mov_b64 v[24:25], v[16:17]\n\t"
    "v_mov_b64 v[26:27], v[16:17]\n\t"
    "v_mov_b64 v[28:29], v[16:17]\n\t"
    "v_mov_b64 v[30:31], v[16:17]\n\t"
    "v_cmp_eq_u32 vcc, 0, v67\n\t"
    "s_nop 1\n\t"
    "v_cndmask_b32 v32, 0, v16, vcc\n\t"
    "v_mov_b32 v33, v32\n\t"
    "v_mov_b64 v[34:35], v[32:33]\n\t"
    "v_mov_b64 v[36:37], v[32:33]\n\t"
    "v_mov_b64 v[38:39], v[32:33]\n\t"
    "v_mov_b64 v[40:41], v[32:33]\n\t"
    "v_mov_b64 v[42:43], v[32:33]\n\t"
    "v_mov_b64 v[44:45], v[32:33]\n\t"
    "v_mov_b64 v[46:47], v[32:33]\n\t"
    "v_mov_b64 v[48:49], v[32:33]\n\t"
    "v_mov_b64 v[50:51], v[32:33]\n\t"
    "v_mov_b64 v[52:53], v[32:33]\n\t"
    "v_mov_b64 v[54:55], v[32:33]\n\t"
    "v_mov_b64 v[56:57], v[32:33]\n\t"
    "v_mov_b64 v[58:59], v[32:33]\n\t"
    "v_mov_b64 v[60:61], v[32:33]\n\t"
    "v_mov_b64 v[62:63], v[32:33]\n\t"
    "v_accvgpr_write_b32 a32, 0\n\t"
    "v_accvgpr_write_b32 a33, 0\n\t"
    "v_accvgpr_write_b32 a34, 0\n\t"
    "v_accvgpr_write_b32 a35, 0\n\t"
    "v_accvgpr_write_b32 a36, 0\n\t"
    "v_accvgpr_write_b32 a37, 0\n\t"
    "v_accvgpr_write_b32 a38, 0\n\t"
    "v_accvgpr_write_b32 a39, 0\n\t"
    "v_accvgpr_write_b32 a40, 0\n\t"
    "v_accvgpr_write_b32 a41, 0\n\t"
    "v_accvgpr_write_b32 a42, 0\n\t"
    "v_accvgpr_write_b32 a43, 0\n\t"
    "v_accvgpr_write_b32 a44, 0\n\t"
    "v_accvgpr_write_b32 a45, 0\n\t"
    "v_accvgpr_write_b32 a46, 0\n\t"
    "v_accvgpr_write_b32 a47, 0\n\t"
    "v_accvgpr_write_b32 a48, 0\n\t"
    "v_accvgpr_write_b32 a49, 0\n\t"
    "v_accvgpr_write_b32 a50, 0\n\t"
    "v_accvgpr_write_b32 a51, 0\n\t"
    "v_accvgpr_write_b32 a52, 0\n\t"
    "v_accvgpr_write_b32 a53, 0\n\t"
    "v_accvgpr_write_b32 a54, 0\n\t"
    "v_accvgpr_write_b32 a55, 0\n\t"
    "v_accvgpr_write_b32 a56, 0\n\t"
    "v_accvgpr_write_b32 a57, 0\n\t"
    "v_accvgpr_write_b32 a58, 0\n\t"
    "v_accvgpr_write_b32 a59, 0\n\t"
    ".LXp183_%=:\n\t"
    "s_cbranch_execz .LXp191_%=\n\t"
    "v_accvgpr_write_b32 a60, 0\n\t"
    "v_accvgpr_write_b32 a61, 0\n\t"
    "v_accvgpr_write_b32 a62, 0\n\t"
    "v_accvgpr_write_b32 a63, 0\n\t"
    "s_lshl_b32 s6, s59, 6\n\t"
    "v_xor_b32 v76, s6, v2\n\t"
    "v_lshlrev_b32 v77, 2, v11\n\t"
    "v_and_b32 v77, 4, v77\n\t"
    "v_xor_b32 v77, v77, v14\n\t"
    "v_lshlrev_b32 v77, 4, v77\n\t"
    "s_movk_i32 s6, 0xc00\n\t"
    "v_mad_u32_u24 v77, v10, s6, v77\n\t"
    "v_xor_b32 v78, 32, v77\n\t"
    "s_lshl_b32 s72, s58, 10\n\t"
    "s_add_u32 s6, s72, 69632\n\t"
    "v_add_u32 v12, s6, v1\n\t"
    "v_mov_b32 v10, 65536\n\t"
    "s_waitcnt vmcnt(0)\n\t"
    "ds_read_b128 a[0:3], v6 offset:0\n\t"
    "ds_read_b64_tr_b16 a[4:5], v9 offset:0\n\t"
    "ds_read_b64_tr_b16 a[6:7], v9 offset:128\n\t"
    "ds_read_b128 a[8:11], v7 offset:0\n\t"
    "ds_read_b64_tr_b16 a[12:13], v9 offset:1024\n\t"
    "ds_read_b64_tr_b16 a[14:15], v9 offset:1152\n\t"
    "ds_read_b128 a[16:19], v6 offset:16384\n\t"
    "ds_read_b64_tr_b16 a[20:21], v9 offset:2048\n\t"
    "ds_read_b64_tr_b16 a[22:23], v9 offset:2176\n\t"
    "ds_read_b128 a[24:27], v7 offset:16384\n\t"
    "ds_read_b64_tr_b16 a[28:29], v9 offset:3072\n\t"
    "ds_read_b64_tr_b16 a[30:31], v9 offset:3200\n\t"
    "v_readfirstlane_b32 s6, v4\n\t"
    "v_readfirstlane_b32 s7, v5\n\t"
    "s_lshr_b64 s[12:13], s[6:7], 29\n\t"
    "s_xor_b64 s[12:13], s[12:13], s[6:7]\n\t"
    "s_mov_b32 s14, 0x7f4a7c15\n\t"
    "s_mov_b32 s15, 0x9e3779b9\n\t"
    "s_mul_i32 s56, s12, s14\n\t"
    "s_mul_hi_u32 s57, s12, s14\n\t"
    "s_mul_i32 s12, s12, s15\n\t"
    "s_mul_i32 s13, s13, s14\n\t"
    "s_add_u32 s57, s57, s12\n\t"
    "s_add_u32 s57, s57, s13\n\t"
    "s_add_u32 s56, s56, 0xd192ed03\n\t"
    "s_addc_u32 s57, s57, 0xd1b54a32\n\t"
    "s_xor_b32 s56, s56, s57\n\t"
    "s_cmp_eq_u32 s56, s6\n\t"
    "s_cselect_b32 s6, 1, 0\n\t"
    "s_xor_b32 s56, s56, s6\n\t"
    "v_mov_b32 v82, s56\n\t"
    "v_mov_b32 v83, s57\n\t"
    "ds_write_b64 v10, v[82:83] offset:136\n\t"
    "s_waitcnt lgkmcnt(1)\n\t"
    "s_bitcmp1_b32 s78, 0\n\t"
    "s_cbranch_scc1 .LX105_%=\n\t"
    "v_mfma_f32_16x16x32_bf16 v[64:67], a[4:7], a[0:3], 0\n\t"
    "v_mfma_f32_16x16x32_bf16 v[64:67], a[12:15], a[8:11], v[64:67]\n\t"
    "v_mfma_f32_16x16x32_bf16 v[64:67], a[20:23], a[16:19], v[64:67]\n\t"
    "v_mfma_f32_16x16x32_bf16 v[64:67], a[28:31], a[24:27], v[64:67]\n\t"
    "s_branch .LX106_%=\n\t"
    ".LX105_%=:\n\t"
    "v_mfma_f32_16x16x32_bf16 v[64:67], a[0:3], a[4:7], 0\n\t"
    "v_mfma_f32_16x16x32_bf16 v[64:67], a[8:11], a[12:15], v[64:67]\n\t"
    "v_mfma_f32_16x16x32_bf16 v[64:67], a[16:19], a[20:23], v[64:67]\n\t"
    "v_mfma_f32_16x16x32_bf16 v[64:67], a[24:27], a[28:31], v[64:67]\n\t"
    ".LX106_%=:\n\t"
    "s_nop 7\n\t"
    "ds_write_b128 v12, v[64:67]\n\t"
    "s_mov_b64 exec, 1\n\t"
    "ds_add_u32 v10, v13 offset:132\n\t"
    "s_mov_b64 exec, -1\n\t"
    "s_cmp_lg_u32 s58, 0\n\t"
    "s_cbranch_scc1 .LX107_%=\n\t"
    "v_and_b32 v6, 0xf0, v1\n\t"
    "v_lshrrev_b32 v7, 5, v0\n\t"
    "s_bitcmp1_b32 s78, 0\n\t"
    "s_cselect_b32 s6, 8, 9\n\t"
    "v_lshlrev_b32 v8, s6, v7\n\t"
    ".LXp184_%=:\n\t"
    "s_cbranch_execz .LXp192_%=\n\t"
    "v_add_u32 v8, v8, v6\n\t"
    "s_lshr_b32 s7, s78, 1\n\t"
    "s_lshl_b32 s8, s7, 9\n\t"
    "s_add_u32 s8, s8, 2048\n\t"
    "s_lshr_b32 s9, s78, 2\n\t"
    "s_lshl_b32 s9, s9, 10\n\t"
    "s_and_b32 s10, s7, 1\n\t"
    "s_lshl_b32 s10, s10, 8\n\t"
    "s_add_u32 s9, s9, s10\n\t"
    "s_bitcmp1_b32 s78, 0\n\t"
    "s_cselect_b32 s8, s8, s9\n\t"
    "s_and_b32 s9, s8, 1023\n\t"
    "s_lshr_b32 s7, s8, 10\n\t"
    "s_lshl_b32 s7, s7, 2\n\t"
    "s_add_u32 s9, s9, s7\n\t"
    "s_lshl_b32 s10, s77, 12\n\t"
    "s_add_u32 s8, s8, s10\n\t"
    "s_add_u32 s8, s8, 65536\n\t"
    "s_lshl_b32 s10, s77, 10\n\t"
    "s_add_u32 s9, s9, s10\n\t"
    "s_add_u32 s9, s9, 32768\n\t"
    "s_mov_b32 s7, 4194304\n\t"
    ".LX108_%=:\n\t"
    "ds_read_b32 v7, v10 offset:132\n\t"
    "ds_read_b128 v[68:71], v12 offset:1024\n\t"
    "ds_read_b128 v[72:75], v12 offset:2048\n\t"
    "ds_read_b128 v[2:5], v12 offset:3072\n\t"
    "s_waitcnt lgkmcnt(0)\n\t"
    "v_cmp_ne_u32 vcc, 4, v7\n\t"
    "s_cbranch_vccz .LX109_%=\n\t"
    "s_sub_u32 s7, s7, 1\n\t"
    "s_cmp_lg_u32 s7, 0\n\t"
    "s_cbranch_scc1 .LX108_%=\n\t"
    ".LX109_%=:\n\t"
    "v_pk_add_f32 v[64:65], v[64:65], v[68:69]\n\t"
    "v_pk_add_f32 v[66:67], v[66:67], v[70:71]\n\t"
    "v_pk_add_f32 v[64:65], v[64:65], v[72:73]\n\t"
    "v_pk_add_f32 v[66:67], v[66:67], v[74:75]\n\t"
    "v_pk_add_f32 v[64:65], v[64:65], v[2:3]\n\t"
    "v_pk_add_f32 v[66:67], v[66:67], v[4:5]\n\t"
    "v_cvt_pk_bf16_f32 v68, v64, v65\n\t"
    "v_cvt_pk_bf16_f32 v69, v66, v67\n\t"
    "v_mov_b32 v70, v68\n\t"
    "v_mov_b32 v71, v69\n\t"
    "s_mov_b32 s6, 0xffff\n\t"
    "s_mov_b32 s7, 0xffff\n\t"
    "v_permlane16_swap_b32 v68, v70\n\t"
    "v_permlane16_swap_b32 v69, v71\n\t"
    "v_add3_u32 v72, v68, v69, v70\n\t"
    "v_add3_u32 v72, v72, v71, s56\n\t"
    "s_mov_b64 exec, s[6:7]\n\t"
    "buffer_store_dwordx4 v[68:71], v8, s[36:39], s8 offen sc1\n\t"
    "buffer_store_dword v72, v8, s[36:39], s9 offen sc1\n\t"
    "s_mov_b64 exec, -1\n\t"
    ".LX107_%=:\n\t"
    "s_mov_b64 s[48:49], s[16:17]\n\t"
    "s_mov_b32 s50, 2097152\n\t"
    "s_mov_b32 s51, 0x20000\n\t"
    "s_mov_b64 s[52:53], s[18:19]\n\t"
    "s_mov_b32 s54, 1572864\n\t"
    "s_mov_b32 s55, 0x20000\n\t"
    "s_mov_b32 s14, 1052289595\n\t"
    "s_add_u32 s6, s72, 73728\n\t"
    "v_add_u32 v2, s6, v1\n\t"
    "s_lshl_b32 s10, s77, 7\n\t"
    "s_lshl_b32 s9, s78, 3\n\t"
    "s_add_u32 s10, s10, s9\n\t"
    "s_add_u32 s10, s10, 4096\n\t"
    "s_lshl_b32 s8, s4, 16\n\t"
    "s_lshl_b32 s6, s58, 13\n\t"
    "s_add_u32 s8, s8, s6\n\t"
    "s_mul_i32 s9, s58, 0x6000\n\t"
    "s_lshl_b32 s7, s5, 7\n\t"
    "s_add_u32 s9, s9, s7\n\t"
    "s_lshl_b32 s7, s78, 7\n\t"
    "s_add_u32 s71, s6, s7\n\t"
    "s_lshl_b32 s12, s5, 1\n\t"
    "s_add_u32 s12, s12, s4\n\t"
    "v_lshlrev_b32 v6, 3, v14\n\t"
    "v_bfe_u32 v7, v0, 3, 1\n\t"
    "v_lshl_add_u32 v6, v7, 7, v6\n\t"
    "v_lshl_add_u32 v6, v11, 10, v6\n\t"
    "s_lshl_b32 s11, s58, 8\n\t"
    ".LXp185_%=:\n\t"
    "s_cbranch_execz .LXp193_%=\n\t"
    "s_add_u32 s11, s11, 4096\n\t"
    "s_lshl_b32 s6, s62, 6\n\t"
    "v_add_u32 v9, s6, v10\n\t"
    "s_lshl_b32 s63, s5, 1\n\t"
    "s_add_u32 s63, s63, s62\n\t"
    "s_lshl_b32 s69, s4, 1\n\t"
    "s_add_u32 s69, s69, s59\n\t"
    "s_lshl_b32 s70, s69, 5\n\t"
    "s_add_u32 s70, s70, s63\n\t"
    "s_lshl_b32 s70, s70, 10\n\t"
    "v_bfe_u32 v7, v0, 4, 1\n\t"
    "v_and_b32 v12, 0xf0, v1\n\t"
    "v_lshl_add_u32 v12, v7, 14, v12\n\t"
    "v_lshrrev_b32 v7, 5, v0\n\t"
    "v_lshl_add_u32 v12, v7, 8, v12\n\t"
    "v_mov_b64 v[72:73], -1\n\t"
    "v_mov_b64 v[74:75], -1\n\t"
    "v_lshrrev_b32 v5, 2, v0\n\t"
    "v_and_b32 v7, 0x30, v1\n\t"
    "v_lshl_add_u32 v4, v5, 11, v7\n\t"
    "v_lshrrev_b32 v7, 1, v7\n\t"
    "v_lshl_add_u32 v5, v5, 9, v7\n\t"
    "v_lshlrev_b32 v8, 1, v11\n\t"
    "v_xor_b32 v8, v8, v14\n\t"
    "v_lshlrev_b32 v8, 4, v8\n\t"
    "v_lshrrev_b32 v7, 3, v0\n\t"
    "v_lshl_add_u32 v8, v7, 10, v8\n\t"
    "v_accvgpr_write_b32 a80, v8\n\t"
    "v_and_b32 v8, 0x70, v77\n\t"
    "v_lshl_add_u32 v8, v7, 10, v8\n\t"
    "v_lshrrev_b32 v7, 5, v0\n\t"
    "v_lshlrev_b32 v7, 9, v7\n\t"
    "v_sub_u32 v11, v81, v7\n\t"
    "s_xor_b32 s6, s59, s62\n\t"
    "s_lshl_b32 s6, s6, 6\n\t"
    "v_xor_b32 v11, s6, v11\n\t"
    "s_lshl_b32 s6, s59, 12\n\t"
    "s_lshl_b32 s6, s6, 3\n\t"
    "s_add_u32 s6, s6, 0x10000\n\t"
    "v_add_u32 v11, s6, v11\n\t"
    "v_mov_b32 v7, 2\n\t"
    "v_add_u32 v64, 0, v80\n\t"
    "v_add_u32 v68, 0, v81\n\t"
    "v_xor_b32 v65, 32, v64\n\t"
    "v_xor_b32 v66, 64, v64\n\t"
    "v_xor_b32 v67, 0x60, v64\n\t"
    "s_waitcnt lgkmcnt(0)\n\t"
    "s_barrier\n\t"
    "ds_read_b128 a[0:3], v64 offset:0\n\t"
    "ds_read_b64_tr_b16 a[4:5], v68 offset:8192\n\t"
    "ds_read_b64_tr_b16 a[6:7], v68 offset:8704\n\t"
    "ds_read_b128 a[8:11], v65 offset:0\n\t"
    "ds_read_b64_tr_b16 a[12:13], v68 offset:10240\n\t"
    "ds_read_b64_tr_b16 a[14:15], v68 offset:10752\n\t"
    "ds_read_b128 a[16:19], v66 offset:0\n\t"
    "ds_read_b64_tr_b16 a[20:21], v68 offset:12288\n\t"
    "ds_read_b64_tr_b16 a[22:23], v68 offset:12800\n\t"
    "ds_read_b128 a[24:27], v67 offset:0\n\t"
    "ds_read_b64_tr_b16 a[28:29], v68 offset:14336\n\t"
    "ds_read_b64_tr_b16 a[30:31], v68 offset:14848\n\t"
    "s_waitcnt lgkmcnt(9)\n\t"
    "v_mfma_f32_32x32x16_bf16 a[64:79], a[4:7], a[0:3], 0\n\t"
    "ds_read_b128 a[0:3], v64 offset:16384\n\t"
    "ds_read_b64_tr_b16 a[4:5], v68 offset:24576\n\t"
    "ds_read_b64_tr_b16 a[6:7], v68 offset:25088\n\t"
    "s_waitcnt lgkmcnt(9)\n\t"
    "v_mfma_f32_32x32x16_bf16 a[64:79], a[12:15], a[8:11], a[64:79]\n\t"
    "ds_read_b128 a[8:11], v65 offset:16384\n\t"
    "ds_read_b64_tr_b16 a[12:13], v68 offset:26624\n\t"
    ".LXp186_%=:\n\t"
    "s_cbranch_execz .LXp194_%=\n\t"
    "ds_read_b64_tr_b16 a[14:15], v68 offset:27136\n\t"
    "s_waitcnt lgkmcnt(9)\n\t"
    "v_mfma_f32_32x32x16_bf16 a[64:79], a[20:23], a[16:19], a[64:79]\n\t"
    "ds_read_b128 a[16:19], v66 offset:16384\n\t"
    "ds_read_b64_tr_b16 a[20:21], v68 offset:28672\n\t"
    "ds_read_b64_tr_b16 a[22:23], v68 offset:29184\n\t"
    "s_waitcnt lgkmcnt(9)\n\t"
    "v_mfma_f32_32x32x16_bf16 a[64:79], a[28:31], a[24:27], a[64:79]\n\t"
    "ds_read_b128 a[24:27], v67 offset:16384\n\t"
    "ds_read_b64_tr_b16 a[28:29], v68 offset:30720\n\t"
    "ds_read_b64_tr_b16 a[30:31], v68 offset:31232\n\t"
    "s_waitcnt lgkmcnt(9)\n\t"
    "v_mfma_f32_32x32x16_bf16 a[64:79], a[4:7], a[0:3], a[64:79]\n\t"
    "s_waitcnt lgkmcnt(6)\n\t"
    "v_mfma_f32_32x32x16_bf16 a[64:79], a[12:15], a[8:11], a[64:79]\n\t"
    "s_waitcnt lgkmcnt(3)\n\t"
    "v_mfma_f32_32x32x16_bf16 a[64:79], a[20:23], a[16:19], a[64:79]\n\t"
    "s_waitcnt lgkmcnt(0)\n\t"
    "v_mfma_f32_32x32x16_bf16 a[64:79], a[28:31], a[24:27], a[64:79]\n\t"
    "v_add_u32 v64, 98304, v80\n\t"
    "v_add_u32 v68, 98304, v81\n\t"
    "v_xor_b32 v65, 32, v64\n\t"
    "v_xor_b32 v66, 64, v64\n\t"
    "v_xor_b32 v67, 0x60, v64\n\t"
    "s_waitcnt lgkmcnt(0)\n\t"
    "s_barrier\n\t"
    "ds_read_b128 a[0:3], v64 offset:0\n\t"
    "ds_read_b64_tr_b16 a[4:5], v68 offset:8192\n\t"
    "ds_read_b64_tr_b16 a[6:7], v68 offset:8704\n\t"
    "ds_read_b128 a[8:11], v65 offset:0\n\t"
    "ds_read_b64_tr_b16 a[12:13], v68 offset:10240\n\t"
    "ds_read_b64_tr_b16 a[14:15], v68 offset:10752\n\t"
    "ds_read_b128 a[16:19], v66 offset:0\n\t"
    "ds_read_b64_tr_b16 a[20:21], v68 offset:12288\n\t"
    "ds_read_b64_tr_b16 a[22:23], v68 offset:12800\n\t"
    "ds_read_b128 a[24:27], v67 offset:0\n\t"
    "ds_read_b64_tr_b16 a[28:29], v68 offset:14336\n\t"
    "ds_read_b64_tr_b16 a[30:31], v68 offset:14848\n\t"
    "s_waitcnt lgkmcnt(9)\n\t"
    "v_mfma_f32_32x32x16_bf16 a[64:79], a[4:7], a[0:3], a[64:79]\n\t"
    "ds_read_b128 a[0:3], v64 offset:16384\n\t"
    "ds_read_b64_tr_b16 a[4:5], v68 offset:24576\n\t"
    "ds_read_b64_tr_b16 a[6:7], v68 offset:25088\n\t"
    "s_waitcnt lgkmcnt(9)\n\t"
    "v_mfma_f32_32x32x16_bf16 a[64:79], a[12:15], a[8:11], a[64:79]\n\t"
    "ds_read_b128 a[8:11], v65 offset:16384\n\t"
    "ds_read_b64_tr_b16 a[12:13], v68 offset:26624\n\t"
    "ds_read_b64_tr_b16 a[14:15], v68 offset:27136\n\t"
    "s_waitcnt lgkmcnt(9)\n\t"
    "v_mfma_f32_32x32x16_bf16 a[64:79], a[20:23], a[16:19], a[64:79]\n\t"
    "ds_read_b128 a[16:19], v66 offset:16384\n\t"
    "ds_read_b64_tr_b16 a[20:21], v68 offset:28672\n\t"
    "ds_read_b64_tr_b16 a[22:23], v68 offset:29184\n\t"
    "s_waitcnt lgkmcnt(9)\n\t"
    "v_mfma_f32_32x32x16_bf16 a[64:79], a[28:31], a[24:27], a[64:79]\n\t"
    "ds_read_b128 a[24:27], v67 offset:16384\n\t"
    "ds_read_b64_tr_b16 a[28:29], v68 offset:30720\n\t"
    "ds_read_b64_tr_b16 a[30:31], v68 offset:31232\n\t"
    "s_waitcnt lgkmcnt(9)\n\t"
    "v_mfma_f32_32x32x16_bf16 a[64:79], a[4:7], a[0:3], a[64:79]\n\t"
    "s_waitcnt lgkmcnt(6)\n\t"
    "v_mfma_f32_32x32x16_bf16 a[64:79], a[12:15], a[8:11], a[64:79]\n\t"
    "s_waitcnt lgkmcnt(3)\n\t"
    "v_mfma_f32_32x32x16_bf16 a[64:79], a[20:23], a[16:19], a[64:79]\n\t"
    "s_waitcnt lgkmcnt(0)\n\t"
    "v_mfma_f32_32x32x16_bf16 a[64:79], a[28:31], a[24:27], a[64:79]\n\t"
    "v_add_u32 v64, 131072, v80\n\t"
    "v_add_u32 v68, 131072, v81\n\t"
    "v_xor_b32 v65, 32, v64\n\t"
    ".LXp187_%=:\n\t"
    "s_cbranch_execz .LXp195_%=\n\t"
    "v_xor_b32 v66, 64, v64\n\t"
    "v_xor_b32 v67, 0x60, v64\n\t"
    "s_waitcnt lgkmcnt(0)\n\t"
    "s_barrier\n\t"
    "ds_read_b128 a[0:3], v64 offset:0\n\t"
    "ds_read_b64_tr_b16 a[4:5], v68 offset:8192\n\t"
    "ds_read_b64_tr_b16 a[6:7], v68 offset:8704\n\t"
    "ds_read_b128 a[8:11], v65 offset:0\n\t"
    "ds_read_b64_tr_b16 a[12:13], v68 offset:10240\n\t"
    "ds_read_b64_tr_b16 a[14:15], v68 offset:10752\n\t"
    "ds_read_b128 a[16:19], v66 offset:0\n\t"
    "ds_read_b64_tr_b16 a[20:21], v68 offset:12288\n\t"
    "ds_read_b64_tr_b16 a[22:23], v68 offset:12800\n\t"
    "ds_read_b128 a[24:27], v67 offset:0\n\t"
    "ds_read_b64_tr_b16 a[28:29], v68 offset:14336\n\t"
    "ds_read_b64_tr_b16 a[30:31], v68 offset:14848\n\t"
    "s_waitcnt lgkmcnt(9)\n\t"
    "v_mfma_f32_32x32x16_bf16 a[64:79], a[4:7], a[0:3], a[64:79]\n\t"
    "ds_read_b128 a[0:3], v64 offset:16384\n\t"
    "ds_read_b64_tr_b16 a[4:5], v68 offset:24576\n\t"
    "ds_read_b64_tr_b16 a[6:7], v68 offset:25088\n\t"
    "s_waitcnt lgkmcnt(9)\n\t"
    "v_mfma_f32_32x32x16_bf16 a[64:79], a[12:15], a[8:11], a[64:79]\n\t"
    "ds_read_b128 a[8:11], v65 offset:16384\n\t"
    "ds_read_b64_tr_b16 a[12:13], v68 offset:26624\n\t"
    "ds_read_b64_tr_b16 a[14:15], v68 offset:27136\n\t"
    "s_waitcnt lgkmcnt(9)\n\t"
    "v_mfma_f32_32x32x16_bf16 a[64:79], a[20:23], a[16:19], a[64:79]\n\t"
    "ds_read_b128 a[16:19], v66 offset:16384\n\t"
    "ds_read_b64_tr_b16 a[20:21], v68 offset:28672\n\t"
    "ds_read_b64_tr_b16 a[22:23], v68 offset:29184\n\t"
    "s_waitcnt lgkmcnt(9)\n\t"
    "v_mfma_f32_32x32x16_bf16 a[64:79], a[28:31], a[24:27], a[64:79]\n\t"
    "ds_read_b128 a[24:27], v67 offset:16384\n\t"
    "ds_read_b64_tr_b16 a[28:29], v68 offset:30720\n\t"
    "ds_read_b64_tr_b16 a[30:31], v68 offset:31232\n\t"
    "s_waitcnt lgkmcnt(9)\n\t"
    "v_mfma_f32_32x32x16_bf16 a[64:79], a[4:7], a[0:3], a[64:79]\n\t"
    "s_waitcnt lgkmcnt(6)\n\t"
    "v_mfma_f32_32x32x16_bf16 a[64:79], a[12:15], a[8:11], a[64:79]\n\t"
    "s_waitcnt lgkmcnt(3)\n\t"
    "v_mfma_f32_32x32x16_bf16 a[64:79], a[20:23], a[16:19], a[64:79]\n\t"
    "s_waitcnt lgkmcnt(0)\n\t"
    "v_mfma_f32_32x32x16_bf16 a[64:79], a[28:31], a[24:27], a[64:79]\n\t"
    "v_add_u32 v64, 32768, v80\n\t"
    "v_add_u32 v68, 32768, v81\n\t"
    "v_xor_b32 v65, 32, v64\n\t"
    "v_xor_b32 v66, 64, v64\n\t"
    "v_xor_b32 v67, 0x60, v64\n\t"
    "s_waitcnt lgkmcnt(0)\n\t"
    "s_barrier\n\t"
    "ds_read_b128 a[0:3], v64 offset:0\n\t"
    "ds_read_b64_tr_b16 a[4:5], v68 offset:8192\n\t"
    "ds_read_b64_tr_b16 a[6:7], v68 offset:8704\n\t"
    "ds_read_b128 a[8:11], v65 offset:0\n\t"
    "ds_read_b64_tr_b16 a[12:13], v68 offset:10240\n\t"
    "ds_read_b64_tr_b16 a[14:15], v68 offset:10752\n\t"
    "ds_read_b128 a[16:19], v66 offset:0\n\t"
    "ds_read_b64_tr_b16 a[20:21], v68 offset:12288\n\t"
    "ds_read_b64_tr_b16 a[22:23], v68 offset:12800\n\t"
    "ds_read_b128 a[24:27], v67 offset:0\n\t"
    "ds_read_b64_tr_b16 a[28:29], v68 offset:14336\n\t"
    "ds_read_b64_tr_b16 a[30:31], v68 offset:14848\n\t"
    "s_waitcnt lgkmcnt(9)\n\t"
    "v_mfma_f32_32x32x16_bf16 a[64:79], a[4:7], a[0:3], a[64:79]\n\t"
    "ds_read_b128 a[0:3], v64 offset:16384\n\t"
    "ds_read_b64_tr_b16 a[4:5], v68 offset:24576\n\t"
    "ds_read_b64_tr_b16 a[6:7], v68 offset:25088\n\t"
    ".LXp188_%=:\n\t"
    "s_cbranch_execz .LXp196_%=\n\t"
    "s_waitcnt lgkmcnt(9)\n\t"
    "v_mfma_f32_32x32x16_bf16 a[64:79], a[12:15], a[8:11], a[64:79]\n\t"
    "ds_read_b128 a[8:11], v65 offset:16384\n\t"
    "ds_read_b64_tr_b16 a[12:13], v68 offset:26624\n\t"
    "ds_read_b64_tr_b16 a[14:15], v68 offset:27136\n\t"
    "s_waitcnt lgkmcnt(9)\n\t"
    "v_mfma_f32_32x32x16_bf16 a[64:79], a[20:23], a[16:19], a[64:79]\n\t"
    "ds_read_b128 a[16:19], v66 offset:16384\n\t"
    "ds_read_b64_tr_b16 a[20:21], v68 offset:28672\n\t"
    "ds_read_b64_tr_b16 a[22:23], v68 offset:29184\n\t"
    "s_waitcnt lgkmcnt(9)\n\t"
    "v_mfma_f32_32x32x16_bf16 a[64:79], a[28:31], a[24:27], a[64:79]\n\t"
    "ds_read_b128 a[24:27], v67 offset:16384\n\t"
    "ds_read_b64_tr_b16 a[28:29], v68 offset:30720\n\t"
    "ds_read_b64_tr_b16 a[30:31], v68 offset:31232\n\t"
    "s_waitcnt lgkmcnt(9)\n\t"
    "v_mfma_f32_32x32x16_bf16 a[64:79], a[4:7], a[0:3], a[64:79]\n\t"
    "s_waitcnt lgkmcnt(6)\n\t"
    "v_mfma_f32_32x32x16_bf16 a[64:79], a[12:15], a[8:11], a[64:79]\n\t"
    "s_waitcnt lgkmcnt(3)\n\t"
    "v_mfma_f32_32x32x16_bf16 a[64:79], a[20:23], a[16:19], a[64:79]\n\t"
    "s_waitcnt lgkmcnt(0)\n\t"
    "v_mfma_f32_32x32x16_bf16 a[64:79], a[28:31], a[24:27], a[64:79]\n\t"
    "s_nop 7\n\t"
    "s_nop 3\n\t"
    "v_accvgpr_read_b32 v64, a64\n\t"
    "v_accvgpr_read_b32 v65, a65\n\t"
    "v_accvgpr_read_b32 v66, a66\n\t"
    "v_accvgpr_read_b32 v67, a67\n\t"
    "v_accvgpr_read_b32 v68, a68\n\t"
    "v_accvgpr_read_b32 v69, a69\n\t"
    "v_accvgpr_read_b32 v70, a70\n\t"
    "v_accvgpr_read_b32 v71, a71\n\t"
    "v_pk_mul_f32 v[64:65], v[64:65], s[14:15] op_sel_hi:[1,0]\n\t"
    "v_pk_mul_f32 v[66:67], v[66:67], s[14:15] op_sel_hi:[1,0]\n\t"
    "v_pk_mul_f32 v[68:69], v[68:69], s[14:15] op_sel_hi:[1,0]\n\t"
    "v_pk_mul_f32 v[70:71], v[70:71], s[14:15] op_sel_hi:[1,0]\n\t"
    "v_cvt_pk_bf16_f32 v64, v64, v65\n\t"
    "v_cvt_pk_bf16_f32 v65, v66, v67\n\t"
    "v_cvt_pk_bf16_f32 v66, v68, v69\n\t"
    "v_cvt_pk_bf16_f32 v67, v70, v71\n\t"
    "ds_write_b128 v2, v[64:67] offset:0\n\t"
    "v_accvgpr_read_b32 v64, a72\n\t"
    "v_accvgpr_read_b32 v65, a73\n\t"
    "v_accvgpr_read_b32 v66, a74\n\t"
    "v_accvgpr_read_b32 v67, a75\n\t"
    "v_accvgpr_read_b32 v68, a76\n\t"
    "v_accvgpr_read_b32 v69, a77\n\t"
    "v_accvgpr_read_b32 v70, a78\n\t"
    "v_accvgpr_read_b32 v71, a79\n\t"
    "v_pk_mul_f32 v[64:65], v[64:65], s[14:15] op_sel_hi:[1,0]\n\t"
    "v_pk_mul_f32 v[66:67], v[66:67], s[14:15] op_sel_hi:[1,0]\n\t"
    "v_pk_mul_f32 v[68:69], v[68:69], s[14:15] op_sel_hi:[1,0]\n\t"
    "v_pk_mul_f32 v[70:71], v[70:71], s[14:15] op_sel_hi:[1,0]\n\t"
    "v_cvt_pk_bf16_f32 v64, v64, v65\n\t"
    "v_cvt_pk_bf16_f32 v65, v66, v67\n\t"
    "v_cvt_pk_bf16_f32 v66, v68, v69\n\t"
    "v_cvt_pk_bf16_f32 v67, v70, v71\n\t"
    "ds_write_b128 v2, v[64:67] offset:4096\n\t"
    "s_cmp_lg_u32 s58, 0\n\t"
    "s_cbranch_scc1 .LX112_%=\n\t"
    "s_waitcnt vmcnt(0)\n\t"
    "s_mov_b64 exec, 1\n\t"
    "buffer_store_dwordx2 v[82:83], off, s[36:39], s10 sc1\n\t"
    "s_mov_b64 exec, -1\n\t"
    ".LX112_%=:\n\t"
    "s_waitcnt lgkmcnt(0)\n\t"
    "s_barrier\n\t"
    "s_barrier\n\t"
    "s_add_u32 m0, s72, 131072\n\t"
    "s_and_b32 s6, s12, 7\n\t"
    ".LXp189_%=:\n\t"
    "s_cbranch_execz .LXp197_%=\n\t"
    "s_mul_i32 s7, s6, 0x30000\n\t"
    "s_lshl_b32 s6, s6, 7\n\t"
    "s_add_u32 s6, s6, s8\n\t"
    "s_add_u32 s7, s7, s9\n\t"
    "buffer_load_dwordx4 v76, s[48:51], s6 offen lds\n\t"
    "s_add_u32 m0, m0, 0x1000\n\t"
    "s_add_u32 s6, s6, 0x8000\n\t"
    "buffer_load_dwordx4 v76, s[48:51], s6 offen lds\n\t"
    "s_add_u32 m0, m0, 0x1000\n\t"
    "s_add_u32 s6, s7, 0x18000\n\t"
    "buffer_load_dwordx4 v77, s[52:55], s7 offen offset:0 lds\n\t"
    "s_add_u32 m0, m0, 0x1000\n\t"
    "s_nop 0\n\t"
    "buffer_load_dwordx4 v77, s[52:55], s6 offen offset:0 lds\n\t"
    "s_add_u32 m0, m0, 0xc00\n\t"
    "s_add_u32 s12, s12, 1\n\t"
    "buffer_load_dwordx4 v77, s[52:55], s7 offen offset:1024 lds\n\t"
    "s_add_u32 m0, m0, 0x1000\n\t"
    "s_nop 0\n\t"
    "buffer_load_dwordx4 v77, s[52:55], s6 offen offset:1024 lds\n\t"
    "s_add_u32 m0, m0, 0xc00\n\t"
    "s_nop 0\n\t"
    "buffer_load_dwordx4 v78, s[52:55], s7 offen offset:2048 lds\n\t"
    "s_add_u32 m0, m0, 0x1000\n\t"
    "s_nop 0\n\t"
    "buffer_load_dwordx4 v78, s[52:55], s6 offen offset:2048 lds\n\t"
    "s_add_u32 m0, m0, 0xc00\n\t"
    "s_nop 0\n\t"
    "buffer_load_dwordx2 v[2:3], v6, s[36:39], s11 offen sc1\n\t"
    "s_add_u32 m0, s72, 98304\n\t"
    "s_and_b32 s6, s12, 7\n\t"
    "s_mul_i32 s7, s6, 0x30000\n\t"
    "s_lshl_b32 s6, s6, 7\n\t"
    "s_add_u32 s6, s6, s8\n\t"
    "s_add_u32 s7, s7, s9\n\t"
    "buffer_load_dwordx4 v76, s[48:51], s6 offen lds\n\t"
    "s_add_u32 m0, m0, 0x1000\n\t"
    "s_add_u32 s6, s6, 0x8000\n\t"
    "buffer_load_dwordx4 v76, s[48:51], s6 offen lds\n\t"
    "s_add_u32 m0, m0, 0x1000\n\t"
    "s_add_u32 s6, s7, 0x18000\n\t"
    "buffer_load_dwordx4 v77, s[52:55], s7 offen offset:0 lds\n\t"
    "s_add_u32 m0, m0, 0x1000\n\t"
    "s_nop 0\n\t"
    "buffer_load_dwordx4 v77, s[52:55], s6 offen offset:0 lds\n\t"
    "s_add_u32 m0, m0, 0xc00\n\t"
    "s_add_u32 s12, s12, 1\n\t"
    "buffer_load_dwordx4 v77, s[52:55], s7 offen offset:1024 lds\n\t"
    "s_add_u32 m0, m0, 0x1000\n\t"
    "s_nop 0\n\t"
    "buffer_load_dwordx4 v77, s[52:55], s6 offen offset:1024 lds\n\t"
    "s_add_u32 m0, m0, 0xc00\n\t"
    "s_nop 0\n\t"
    "buffer_load_dwordx4 v78, s[52:55], s7 offen offset:2048 lds\n\t"
    "s_add_u32 m0, m0, 0x1000\n\t"
    "s_nop 0\n\t"
    "buffer_load_dwordx4 v78, s[52:55], s6 offen offset:2048 lds\n\t"
    "s_add_u32 m0, m0, 0xc00\n\t"
    "s_nop 0\n\t"
    "s_mov_b32 s14, 524288\n\t"
    "s_waitcnt vmcnt(8)\n\t"
    ".LX113_%=:\n\t"
    "v_cmp_ne_u64 vcc, v[2:3], v[82:83]\n\t"
    "s_cbranch_vccz .LX114_%=\n\t"
    "buffer_load_dwordx2 v[2:3], v6, s[36:39], s11 offen sc1\n\t"
    "s_waitcnt vmcnt(0)\n\t"
    "s_sub_u32 s14, s14, 1\n\t"
    "s_cmp_lg_u32 s14, 0\n\t"
    "s_cbranch_scc1 .LX113_%=\n\t"
    ".LX114_%=:\n\t"
    "s_add_u32 s6, s4, 8\n\t"
    "s_and_b32 s6, s6, 24\n\t"
    "s_lshl_b32 s6, s6, 12\n\t"
    "s_lshl_b32 s7, s58, 13\n\t"
    "s_add_u32 m0, s7, 0x8000\n\t"
    "s_add_u32 s6, s6, s7\n\t"
    "s_add_u32 s6, s6, 65536\n\t"
    "buffer_load_dwordx4 v1, s[36:39], s6 offen offset:0 sc1 lds\n\t"
    ".LXp190_%=:\n\t"
    "s_cbranch_execz .LXp198_%=\n\t"
    "buffer_load_dwordx4 v1, s[36:39], s6 offen offset:1024 sc1 lds\n\t"
    "buffer_load_dwordx4 v1, s[36:39], s6 offen offset:2048 sc1 lds\n\t"
    "buffer_load_dwordx4 v1, s[36:39], s6 offen offset:3072 sc1 lds\n\t"
    "s_add_u32 m0, m0, 0x1000\n\t"
    "s_add_u32 s6, s6, 0x1000\n\t"
    "buffer_load_dwordx4 v1, s[36:39], s6 offen offset:0 sc1 lds\n\t"
    "buffer_load_dwordx4 v1, s[36:39], s6 offen offset:1024 sc1 lds\n\t"
    "buffer_load_dwordx4 v1, s[36:39], s6 offen offset:2048 sc1 lds\n\t"
    "buffer_load_dwordx4 v1, s[36:39], s6 offen offset:3072 sc1 lds\n\t"
    "s_add_u32 m0, s72, 73728\n\t"
    "s_and_b32 s6, s12, 7\n\t"
    "s_mul_i32 s7, s6, 0x30000\n\t"
    "s_lshl_b32 s6, s6, 7\n\t"
    "s_add_u32 s6, s6, s8\n\t"
    "s_add_u32 s7, s7, s9\n\t"
    "s_add_u32 s6, s7, 0x18000\n\t"
    "buffer_load_dwordx4 v77, s[52:55], s7 offen offset:0 lds\n\t"
    "s_add_u32 m0, m0, 0x1000\n\t"
    "s_nop 0\n\t"
    "buffer_load_dwordx4 v77, s[52:55], s6 offen offset:0 lds\n\t"
    "s_add_u32 m0, m0, 0xc00\n\t"
    "s_add_u32 s12, s12, 1\n\t"
    "buffer_load_dwordx4 v77, s[52:55], s7 offen offset:1024 lds\n\t"
    "s_add_u32 m0, m0, 0x1000\n\t"
    "s_nop 0\n\t"
    "buffer_load_dwordx4 v77, s[52:55], s6 offen offset:1024 lds\n\t"
    "s_add_u32 m0, m0, 0xc00\n\t"
    "s_nop 0\n\t"
    "buffer_load_dwordx4 v78, s[52:55], s7 offen offset:2048 lds\n\t"
    "s_add_u32 m0, m0, 0x1000\n\t"
    "s_nop 0\n\t"
    "buffer_load_dwordx4 v78, s[52:55], s6 offen offset:2048 lds\n\t"
    "s_add_u32 m0, m0, 0xc00\n\t"
    "s_nop 0\n\t"
    "s_add_u32 s10, s70, 3145728\n\t"
    "buffer_store_dwordx4 v[72:75], v12, s[36:39], s10 offen sc1\n\t"
    "buffer_store_dwordx4 v[72:75], v12, s[36:39], s10 offen offset:512 sc1\n\t"
    "s_waitcnt vmcnt(8)\n\t"
    "s_mov_b64 exec, 1\n\t"
    "ds_add_u32 v9, v7 offset:4\n\t"
    "s_mov_b64 exec, -1\n\t"
    "s_mov_b32 s14, 8\n\t"
    "s_mov_b32 s13, 0x20000\n\t"
    "s_mov_b32 s15, 0x20000\n\t"
    "s_mov_b32 s7, 4194304\n\t"
    ".LXw115_%=:\n\t"
    "ds_read_b32 v14, v10 offset:32\n\t"
    "s_waitcnt lgkmcnt(0)\n\t"
    "v_readfirstlane_b32 s6, v14\n\t"
    "s_cmp_ge_u32 s6, 4\n\t"
    "s_cbranch_scc1 .LXd116_%=\n\t"
    "s_sleep 2\n\t"
    "s_sub_u32 s7, s7, 1\n\t"
    "s_cmp_lg_u32 s7, 0\n\t"
    "s_cbranch_scc1 .LXw115_%=\n\t"
    ".LXd116_%=:\n\t"
    "s_mov_b32 m0, s72\n\t"
    "s_add_u32 s6, s12, 7\n\t"
    "s_and_b32 s6, s6, 7\n\t"
    "s_lshl_b32 s6, s6, 7\n\t"
    "s_add_u32 s6, s6, s8\n\t"
    "buffer_load_dwordx4 v76, s[48:51], s6 offen lds\n\t"
    "s_add_u32 m0, m0, 0x1000\n\t"
    "s_add_u32 s6, s6, 0x8000\n\t"
    "buffer_load_dwordx4 v76, s[48:51], s6 offen lds\n\t"
    "s_add_u32 m0, s72, 8192\n\t"
    "s_and_b32 s6, s12, 7\n\t"
    "s_mul_i32 s7, s6, 0x30000\n\t"
    "s_lshl_b32 s6, s6, 7\n\t"
    "s_add_u32 s6, s6, s8\n\t"
    "s_add_u32 s7, s7, s9\n\t"
    "s_add_u32 s6, s7, 0x18000\n\t"
    "buffer_load_dwordx4 v77, s[52:55], s7 offen offset:0 lds\n\t"
    "s_add_u32 m0, m0, 0x1000\n\t"
    "s_nop 0\n\t"
    "buffer_load_dwordx4 v77, s[52:55], s6 offen offset:0 lds\n\t"
    "s_waitcnt vmcnt(6)\n\t"
    "v_and_b32 v80, 0xfff, v80\n\t"
    "v_add_u32 v64, s13, v80\n\t"
    ".LXp191_%=:\n\t"
    "s_cbranch_execz .LXp199_%=\n\t"
    "v_xor_b32 v65, 32, v64\n\t"
    "v_xor_b32 v66, 64, v64\n\t"
    "v_xor_b32 v67, 0x60, v64\n\t"
    "v_add_u32 v68, s15, v81\n\t"
    "v_add_u32 v69, s15, v79\n\t"
    "s_mov_b32 s64, 58818560\n\t"
    "s_mov_b32 s65, 7\n\t"
    "s_mov_b32 s66, 2617868288\n\t"
    "s_mov_b32 s67, 0\n\t"
    "s_cmp_lg_u32 s59, 0\n\t"
    "s_cbranch_scc1 .LX120_%=\n\t"
    "s_branch .LX119_%=\n\t"
    ".p2align 9\n\t"
    ".LX121_%=:\n\t"
    "v_mfma_f32_32x32x16_bf16 v[48:63], a[20:23], a[28:31], v[48:63]\n\t"
    "v_xor_b32 v67, 0x60, v64\n\t"
    "v_add_u32 v68, s15, v81\n\t"
    "v_add_u32 v69, s15, v79\n\t"
    "s_waitcnt vmcnt(0) lgkmcnt(0)\n\t"
    "v_mfma_f32_32x32x16_bf16 v[16:31], a[24:27], a[16:19], v[16:31]\n\t"
    ".LX119_%=:\n\t"
    "s_barrier\n\t"
    "v_mfma_f32_32x32x16_bf16 v[32:47], a[32:35], a[44:47], v[32:47]\n\t"
    "ds_read_b128 a[0:3], v64\n\t"
    "ds_read_b64_tr_b16 a[12:13], v69 offset:16384\n\t"
    "ds_read_b64_tr_b16 a[14:15], v69 offset:16896\n\t"
    "v_mfma_f32_32x32x16_bf16 v[48:63], a[36:39], a[44:47], v[48:63]\n\t"
    "ds_read_b128 a[4:7], v64 offset:4096\n\t"
    "ds_read_b64_tr_b16 a[8:9], v68 offset:8192\n\t"
    "ds_read_b64_tr_b16 a[10:11], v68 offset:8704\n\t"
    "v_mfma_f32_32x32x16_bf16 v[16:31], a[40:43], a[32:35], v[16:31]\n\t"
    "ds_read_b128 a[16:19], v65\n\t"
    "ds_read_b64_tr_b16 a[28:29], v69 offset:18432\n\t"
    "ds_read_b64_tr_b16 a[30:31], v69 offset:18944\n\t"
    "v_mfma_f32_32x32x16_bf16 v[32:47], a[48:51], a[60:63], v[32:47]\n\t"
    "ds_read_b128 a[20:23], v65 offset:4096\n\t"
    "ds_read_b64_tr_b16 a[24:25], v68 offset:10240\n\t"
    "ds_read_b64_tr_b16 a[26:27], v68 offset:10752\n\t"
    "v_mfma_f32_32x32x16_bf16 v[48:63], a[52:55], a[60:63], v[48:63]\n\t"
    "ds_read_b128 a[32:35], v66\n\t"
    "ds_read_b64_tr_b16 a[44:45], v69 offset:20480\n\t"
    "ds_read_b64_tr_b16 a[46:47], v69 offset:20992\n\t"
    "v_mfma_f32_32x32x16_bf16 v[16:31], a[56:59], a[48:51], v[16:31]\n\t"
    "ds_read_b128 a[36:39], v66 offset:4096\n\t"
    "ds_read_b64_tr_b16 a[40:41], v68 offset:12288\n\t"
    "ds_read_b64_tr_b16 a[42:43], v68 offset:12800\n\t"
    "s_waitcnt lgkmcnt(12)\n\t"
    "v_mfma_f32_32x32x16_bf16 v[32:47], a[0:3], a[12:15], v[32:47]\n\t"
    "ds_read_b128 a[48:51], v67\n\t"
    "ds_read_b64_tr_b16 a[60:61], v69 offset:22528\n\t"
    "ds_read_b64_tr_b16 a[62:63], v69 offset:23040\n\t"
    "s_and_b32 s13, s64, 0x38000\n\t"
    "v_mfma_f32_32x32x16_bf16 v[48:63], a[4:7], a[12:15], v[48:63]\n\t"
    "ds_read_b128 a[52:55], v67 offset:4096\n\t"
    "ds_read_b64_tr_b16 a[56:57], v68 offset:14336\n\t"
    "ds_read_b64_tr_b16 a[58:59], v68 offset:14848\n\t"
    "s_and_b32 s15, s66, 0x38000\n\t"
    "v_mfma_f32_32x32x16_bf16 v[16:31], a[8:11], a[0:3], v[16:31]\n\t"
    "v_add_u32 v64, s13, v80\n\t"
    "v_xor_b32 v65, 32, v64\n\t"
    "v_xor_b32 v66, 64, v64\n\t"
    "s_lshr_b64 s[64:65], s[64:65], 3\n\t"
    "s_lshr_b64 s[66:67], s[66:67], 3\n\t"
    "s_waitcnt lgkmcnt(12)\n\t"
    "v_mfma_f32_32x32x16_bf16 v[32:47], a[16:19], a[28:31], v[32:47]\n\t"
    "s_sub_u32 s14, s14, 1\n\t"
    "s_cmp_lg_u32 s14, 0\n\t"
    "s_cbranch_scc1 .LX121_%=\n\t"
    "v_mfma_f32_32x32x16_bf16 v[48:63], a[20:23], a[28:31], v[48:63]\n\t"
    "s_waitcnt lgkmcnt(0)\n\t"
    "v_mfma_f32_32x32x16_bf16 v[16:31], a[24:27], a[16:19], v[16:31]\n\t"
    "s_memrealtime s[74:75]\n\t"
    "s_mov_b64 exec, 1\n\t"
    "ds_add_u32 v10, v13 offset:148\n\t"
    "s_mov_b64 exec, -1\n\t"
    "s_nop 3\n\t"
    "v_mfma_f32_32x32x16_bf16 v[16:31], a[40:43], a[32:35], v[16:31]\n\t"
    "v_mfma_f32_32x32x16_bf16 v[16:31], a[56:59], a[48:51], v[16:31]\n\t"
    "s_branch .LX118_%=\n\t"
    ".p2align 9\n\t"
    ".LX122_%=:\n\t"
    "v_mfma_f32_32x32x16_bf16 v[48:63], a[20:23], a[28:31], v[48:63]\n\t"
    "v_xor_b32 v67, 0x60, v64\n\t"
    "v_add_u32 v68, s15, v81\n\t"
    "v_add_u32 v69, s15, v79\n\t"
    "s_waitcnt vmcnt(0) lgkmcnt(0)\n\t"
    "v_mfma_f32_32x32x16_bf16 v[16:31], a[24:27], a[20:23], v[16:31]\n\t"
    ".LX120_%=:\n\t"
    "s_barrier\n\t"
    "v_mfma_f32_32x32x16_bf16 v[32:47], a[32:35], a[44:47], v[32:47]\n\t"
    "ds_read_b128 a[0:3], v64\n\t"
    "ds_read_b64_tr_b16 a[12:13], v69 offset:16384\n\t"
    "ds_read_b64_tr_b16 a[14:15], v69 offset:16896\n\t"
    "v_mfma_f32_32x32x16_bf16 v[48:63], a[36:39], a[44:47], v[48:63]\n\t"
    "ds_read_b128 a[4:7], v64 offset:4096\n\t"
    "ds_read_b64_tr_b16 a[8:9], v68 offset:8192\n\t"
    "ds_read_b64_tr_b16 a[10:11], v68 offset:8704\n\t"
    "v_mfma_f32_32x32x16_bf16 v[16:31], a[40:43], a[36:39], v[16:31]\n\t"
    "ds_read_b128 a[16:19], v65\n\t"
    "ds_read_b64_tr_b16 a[28:29], v69 offset:18432\n\t"
    "ds_read_b64_tr_b16 a[30:31], v69 offset:18944\n\t"
    "v_mfma_f32_32x32x16_bf16 v[32:47], a[48:51], a[60:63], v[32:47]\n\t"
    "ds_read_b128 a[20:23], v65 offset:4096\n\t"
    "ds_read_b64_tr_b16 a[24:25], v68 offset:10240\n\t"
    "ds_read_b64_tr_b16 a[26:27], v68 offset:10752\n\t"
    "v_mfma_f32_32x32x16_bf16 v[48:63], a[52:55], a[60:63], v[48:63]\n\t"
    "ds_read_b128 a[32:35], v66\n\t"
    "ds_read_b64_tr_b16 a[44:45], v69 offset:20480\n\t"
    "ds_read_b64_tr_b16 a[46:47], v69 offset:20992\n\t"
    "v_mfma_f32_32x32x16_bf16 v[16:31], a[56:59], a[52:55], v[16:31]\n\t"
    "ds_read_b128 a[36:39], v66 offset:4096\n\t"
    "ds_read_b64_tr_b16 a[40:41], v68 offset:12288\n\t"
    "ds_read_b64_tr_b16 a[42:43], v68 offset:12800\n\t"
    "s_waitcnt lgkmcnt(12)\n\t"
    "v_mfma_f32_32x32x16_bf16 v[32:47], a[0:3], a[12:15], v[32:47]\n\t"
    "ds_read_b128 a[48:51], v67\n\t"
    "ds_read_b64_tr_b16 a[60:61], v69 offset:22528\n\t"
    "ds_read_b64_tr_b16 a[62:63], v69 offset:23040\n\t"
    "s_and_b32 s13, s64, 0x38000\n\t"
    "v_mfma_f32_32x32x16_bf16 v[48:63], a[4:7], a[12:15], v[48:63]\n\t"
    "ds_read_b128 a[52:55], v67 offset:4096\n\t"
    "ds_read_b64_tr_b16 a[56:57], v68 offset:14336\n\t"
    "ds_read_b64_tr_b16 a[58:59], v68 offset:14848\n\t"
    "s_and_b32 s15, s66, 0x38000\n\t"
    "v_mfma_f32_32x32x16_bf16 v[16:31], a[8:11], a[4:7], v[16:31]\n\t"
    "v_add_u32 v64, s13, v80\n\t"
    "v_xor_b32 v65, 32, v64\n\t"
    "v_xor_b32 v66, 64, v64\n\t"
    "s_lshr_b64 s[64:65], s[64:65], 3\n\t"
    "s_lshr_b64 s[66:67], s[66:67], 3\n\t"
    "s_waitcnt lgkmcnt(12)\n\t"
    "v_mfma_f32_32x32x16_bf16 v[32:47], a[16:19], a[28:31], v[32:47]\n\t"
    "s_sub_u32 s14, s14, 1\n\t"
    "s_cmp_lg_u32 s14, 0\n\t"
    "s_cbranch_scc1 .LX122_%=\n\t"
    "v_mfma_f32_32x32x16_bf16 v[48:63], a[20:23], a[28:31], v[48:63]\n\t"
    "s_waitcnt lgkmcnt(0)\n\t"
    "v_mfma_f32_32x32x16_bf16 v[16:31], a[24:27], a[20:23], v[16:31]\n\t"
    "s_memrealtime s[74:75]\n\t"
    "s_mov_b64 exec, 1\n\t"
    "ds_add_u32 v10, v13 offset:148\n\t"
    "s_mov_b64 exec, -1\n\t"
    "s_nop 3\n\t"
    "v_mfma_f32_32x32x16_bf16 v[16:31], a[40:43], a[36:39], v[16:31]\n\t"
    "v_mfma_f32_32x32x16_bf16 v[16:31], a[56:59], a[52:55], v[16:31]\n\t"
    ".LX118_%=:\n\t"
    "v_mfma_f32_32x32x16_bf16 v[32:47], a[32:35], a[44:47], v[32:47]\n\t"
    "v_mfma_f32_32x32x16_bf16 v[32:47], a[48:51], a[60:63], v[32:47]\n\t"
    "v_mfma_f32_32x32x16_bf16 v[48:63], a[36:39], a[44:47], v[48:63]\n\t"
    "v_mfma_f32_32x32x16_bf16 v[48:63], a[52:55], a[60:63], v[48:63]\n\t"
    "s_mov_b32 s7, 4194304\n\t"
    ".LXw123_%=:\n\t"
    ".LXp192_%=:\n\t"
    "s_cbranch_execz .LXp200_%=\n\t"
    "ds_read_b32 v14, v10 offset:148\n\t"
    "s_waitcnt lgkmcnt(0)\n\t"
    "v_readfirstlane_b32 s6, v14\n\t"
    "s_cmp_ge_u32 s6, 4\n\t"
    "s_cbranch_scc1 .LXd124_%=\n\t"
    "s_sleep 1\n\t"
    "s_sub_u32 s7, s7, 1\n\t"
    "s_cmp_lg_u32 s7, 0\n\t"
    "s_cbranch_scc1 .LXw123_%=\n\t"
    ".LXd124_%=:\n\t"
    "s_add_u32 s6, s4, 16\n\t"
    "s_and_b32 s6, s6, 24\n\t"
    "s_lshl_b32 s6, s6, 12\n\t"
    "s_lshl_b32 s7, s58, 13\n\t"
    "s_add_u32 m0, s7, 0\n\t"
    "s_add_u32 s6, s6, s7\n\t"
    "s_add_u32 s6, s6, 65536\n\t"
    "buffer_load_dwordx4 v1, s[36:39], s6 offen offset:0 sc1 lds\n\t"
    "buffer_load_dwordx4 v1, s[36:39], s6 offen offset:1024 sc1 lds\n\t"
    "buffer_load_dwordx4 v1, s[36:39], s6 offen offset:2048 sc1 lds\n\t"
    "buffer_load_dwordx4 v1, s[36:39], s6 offen offset:3072 sc1 lds\n\t"
    "s_add_u32 m0, m0, 0x1000\n\t"
    "s_add_u32 s6, s6, 0x1000\n\t"
    "buffer_load_dwordx4 v1, s[36:39], s6 offen offset:0 sc1 lds\n\t"
    "buffer_load_dwordx4 v1, s[36:39], s6 offen offset:1024 sc1 lds\n\t"
    "buffer_load_dwordx4 v1, s[36:39], s6 offen offset:2048 sc1 lds\n\t"
    "buffer_load_dwordx4 v1, s[36:39], s6 offen offset:3072 sc1 lds\n\t"
    "s_add_u32 s6, s4, 24\n\t"
    "s_and_b32 s6, s6, 24\n\t"
    "s_lshl_b32 s6, s6, 12\n\t"
    "s_lshl_b32 s7, s58, 13\n\t"
    "s_add_u32 m0, s7, 0x20000\n\t"
    "s_add_u32 s6, s6, s7\n\t"
    "s_add_u32 s6, s6, 65536\n\t"
    "buffer_load_dwordx4 v1, s[36:39], s6 offen offset:0 sc1 lds\n\t"
    "buffer_load_dwordx4 v1, s[36:39], s6 offen offset:1024 sc1 lds\n\t"
    "buffer_load_dwordx4 v1, s[36:39], s6 offen offset:2048 sc1 lds\n\t"
    "buffer_load_dwordx4 v1, s[36:39], s6 offen offset:3072 sc1 lds\n\t"
    "s_add_u32 m0, m0, 0x1000\n\t"
    "s_add_u32 s6, s6, 0x1000\n\t"
    "buffer_load_dwordx4 v1, s[36:39], s6 offen offset:0 sc1 lds\n\t"
    "buffer_load_dwordx4 v1, s[36:39], s6 offen offset:1024 sc1 lds\n\t"
    "buffer_load_dwordx4 v1, s[36:39], s6 offen offset:2048 sc1 lds\n\t"
    "buffer_load_dwordx4 v1, s[36:39], s6 offen offset:3072 sc1 lds\n\t"
    "s_bitcmp1_b32 s2, 7\n\t"
    "s_cselect_b64 s[40:41], s[30:31], s[20:21]\n\t"
    "s_mov_b32 s42, 0x80000\n\t"
    "s_bfe_u32 s6, s2, 0x40003\n\t"
    "s_lshl_b32 s6, s6, 2\n\t"
    "s_add_u32 s6, s6, s58\n\t"
    "s_lshl_b32 s6, s6, 13\n\t"
    "v_lshlrev_b32 v64, 7, v0\n\t"
    "s_mov_b32 m0, 65792\n\t"
    "s_mov_b32 s12, 1069066811\n\t"
    "s_mov_b32 s10, 897988541\n\t"
    "v_pk_mul_f32 v[64:65], v[16:17], s[12:13] op_sel_hi:[1,0]\n\t"
    "v_pk_mul_f32 v[66:67], v[18:19], s[12:13] op_sel_hi:[1,0]\n\t"
    "v_pk_mul_f32 v[68:69], v[20:21], s[12:13] op_sel_hi:[1,0]\n\t"
    "v_pk_mul_f32 v[70:71], v[22:23], s[12:13] op_sel_hi:[1,0]\n\t"
    "v_pk_mul_f32 v[72:73], v[24:25], s[12:13] op_sel_hi:[1,0]\n\t"
    "v_pk_mul_f32 v[74:75], v[26:27], s[12:13] op_sel_hi:[1,0]\n\t"
    "v_pk_mul_f32 v[76:77], v[28:29], s[12:13] op_sel_hi:[1,0]\n\t"
    "v_pk_mul_f32 v[78:79], v[30:31], s[12:13] op_sel_hi:[1,0]\n\t"
    "v_exp_f32_e64 v64, v64 clamp\n\t"
    "v_max_f32 v16, s10, v16\n\t"
    "v_exp_f32_e64 v65, v65 clamp\n\t"
    "v_max_f32 v17, s10, v17\n\t"
    "v_exp_f32_e64 v66, v66 clamp\n\t"
    "v_max_f32 v18, s10, v18\n\t"
    "v_exp_f32_e64 v67, v67 clamp\n\t"
    "v_max_f32 v19, s10, v19\n\t"
    "v_exp_f32_e64 v68, v68 clamp\n\t"
    "v_max_f32 v20, s10, v20\n\t"
    "v_exp_f32_e64 v69, v69 clamp\n\t"
    ".LXp193_%=:\n\t"
    "s_cbranch_execz .LXp201_%=\n\t"
    "v_max_f32 v21, s10, v21\n\t"
    "v_exp_f32_e64 v70, v70 clamp\n\t"
    "v_max_f32 v22, s10, v22\n\t"
    "v_exp_f32_e64 v71, v71 clamp\n\t"
    "v_max_f32 v23, s10, v23\n\t"
    "v_exp_f32_e64 v72, v72 clamp\n\t"
    "v_max_f32 v24, s10, v24\n\t"
    "v_exp_f32_e64 v73, v73 clamp\n\t"
    "v_max_f32 v25, s10, v25\n\t"
    "v_exp_f32_e64 v74, v74 clamp\n\t"
    "v_max_f32 v26, s10, v26\n\t"
    "v_exp_f32_e64 v75, v75 clamp\n\t"
    "v_max_f32 v27, s10, v27\n\t"
    "v_exp_f32_e64 v76, v76 clamp\n\t"
    "v_max_f32 v28, s10, v28\n\t"
    "v_exp_f32_e64 v77, v77 clamp\n\t"
    "v_max_f32 v29, s10, v29\n\t"
    "v_exp_f32_e64 v78, v78 clamp\n\t"
    "v_max_f32 v30, s10, v30\n\t"
    "v_exp_f32_e64 v79, v79 clamp\n\t"
    "v_max_f32 v31, s10, v31\n\t"
    "v_pk_add_f32 v[16:17], v[16:17], v[64:65]\n\t"
    "v_pk_add_f32 v[18:19], v[18:19], v[66:67]\n\t"
    "v_pk_add_f32 v[20:21], v[20:21], v[68:69]\n\t"
    "v_pk_add_f32 v[22:23], v[22:23], v[70:71]\n\t"
    "v_pk_add_f32 v[24:25], v[24:25], v[72:73]\n\t"
    "v_pk_add_f32 v[26:27], v[26:27], v[74:75]\n\t"
    "v_pk_add_f32 v[28:29], v[28:29], v[76:77]\n\t"
    "v_pk_add_f32 v[30:31], v[30:31], v[78:79]\n\t"
    "v_cvt_pk_bf16_f32 v16, v16, v17\n\t"
    "v_cvt_pk_bf16_f32 v17, v18, v19\n\t"
    "v_cvt_pk_bf16_f32 v18, v20, v21\n\t"
    "v_cvt_pk_bf16_f32 v19, v22, v23\n\t"
    "v_cvt_pk_bf16_f32 v20, v24, v25\n\t"
    "v_cvt_pk_bf16_f32 v21, v26, v27\n\t"
    "v_cvt_pk_bf16_f32 v22, v28, v29\n\t"
    "v_cvt_pk_bf16_f32 v23, v30, v31\n\t"
    "v_permlane16_swap_b32 v32, v48\n\t"
    "v_permlane16_swap_b32 v33, v49\n\t"
    "v_permlane16_swap_b32 v34, v50\n\t"
    "v_permlane16_swap_b32 v35, v51\n\t"
    "v_permlane16_swap_b32 v36, v52\n\t"
    "v_permlane16_swap_b32 v37, v53\n\t"
    "v_permlane16_swap_b32 v38, v54\n\t"
    "v_permlane16_swap_b32 v39, v55\n\t"
    "v_permlane16_swap_b32 v40, v56\n\t"
    "v_permlane16_swap_b32 v41, v57\n\t"
    "v_permlane16_swap_b32 v42, v58\n\t"
    "v_permlane16_swap_b32 v43, v59\n\t"
    "v_permlane16_swap_b32 v44, v60\n\t"
    "v_permlane16_swap_b32 v45, v61\n\t"
    "v_permlane16_swap_b32 v46, v62\n\t"
    "v_permlane16_swap_b32 v47, v63\n\t"
    "v_pk_mul_f32 v[64:65], v[32:33], s[12:13] op_sel_hi:[1,0]\n\t"
    "v_pk_mul_f32 v[66:67], v[34:35], s[12:13] op_sel_hi:[1,0]\n\t"
    "v_pk_mul_f32 v[68:69], v[36:37], s[12:13] op_sel_hi:[1,0]\n\t"
    "v_pk_mul_f32 v[70:71], v[38:39], s[12:13] op_sel_hi:[1,0]\n\t"
    "v_pk_mul_f32 v[72:73], v[40:41], s[12:13] op_sel_hi:[1,0]\n\t"
    "v_pk_mul_f32 v[74:75], v[42:43], s[12:13] op_sel_hi:[1,0]\n\t"
    "v_pk_mul_f32 v[76:77], v[44:45], s[12:13] op_sel_hi:[1,0]\n\t"
    "v_pk_mul_f32 v[78:79], v[46:47], s[12:13] op_sel_hi:[1,0]\n\t"
    ".LXp194_%=:\n\t"
    "s_cbranch_execz .LXp202_%=\n\t"
    "v_exp_f32_e64 v64, v64 clamp\n\t"
    "v_max_f32 v32, s10, v32\n\t"
    "v_exp_f32_e64 v65, v65 clamp\n\t"
    "v_max_f32 v33, s10, v33\n\t"
    "v_exp_f32_e64 v66, v66 clamp\n\t"
    "v_max_f32 v34, s10, v34\n\t"
    "v_exp_f32_e64 v67, v67 clamp\n\t"
    "v_max_f32 v35, s10, v35\n\t"
    "v_exp_f32_e64 v68, v68 clamp\n\t"
    "v_max_f32 v36, s10, v36\n\t"
    "v_exp_f32_e64 v69, v69 clamp\n\t"
    "v_max_f32 v37, s10, v37\n\t"
    "v_exp_f32_e64 v70, v70 clamp\n\t"
    "v_max_f32 v38, s10, v38\n\t"
    "v_exp_f32_e64 v71, v71 clamp\n\t"
    "v_max_f32 v39, s10, v39\n\t"
    "v_exp_f32_e64 v72, v72 clamp\n\t"
    "v_max_f32 v40, s10, v40\n\t"
    "v_exp_f32_e64 v73, v73 clamp\n\t"
    "v_max_f32 v41, s10, v41\n\t"
    "v_exp_f32_e64 v74, v74 clamp\n\t"
    "v_max_f32 v42, s10, v42\n\t"
    "v_exp_f32_e64 v75, v75 clamp\n\t"
    "v_max_f32 v43, s10, v43\n\t"
    "v_exp_f32_e64 v76, v76 clamp\n\t"
    "v_max_f32 v44, s10, v44\n\t"
    "v_exp_f32_e64 v77, v77 clamp\n\t"
    "v_max_f32 v45, s10, v45\n\t"
    "v_exp_f32_e64 v78, v78 clamp\n\t"
    "v_max_f32 v46, s10, v46\n\t"
    "v_exp_f32_e64 v79, v79 clamp\n\t"
    "v_max_f32 v47, s10, v47\n\t"
    "v_pk_add_f32 v[32:33], v[32:33], v[64:65]\n\t"
    "v_pk_add_f32 v[34:35], v[34:35], v[66:67]\n\t"
    "v_pk_add_f32 v[36:37], v[36:37], v[68:69]\n\t"
    "v_pk_add_f32 v[38:39], v[38:39], v[70:71]\n\t"
    "v_pk_add_f32 v[40:41], v[40:41], v[72:73]\n\t"
    "v_pk_add_f32 v[42:43], v[42:43], v[74:75]\n\t"
    "v_pk_add_f32 v[44:45], v[44:45], v[76:77]\n\t"
    "v_pk_add_f32 v[46:47], v[46:47], v[78:79]\n\t"
    "v_cvt_pk_bf16_f32 v64, v32, v33\n\t"
    "v_cvt_pk_bf16_f32 v65, v34, v35\n\t"
    "v_cvt_pk_bf16_f32 v66, v36, v37\n\t"
    "v_cvt_pk_bf16_f32 v67, v38, v39\n\t"
    "v_cvt_pk_bf16_f32 v68, v40, v41\n\t"
    "v_cvt_pk_bf16_f32 v69, v42, v43\n\t"
    "v_cvt_pk_bf16_f32 v70, v44, v45\n\t"
    "v_cvt_pk_bf16_f32 v71, v46, v47\n\t"
    "v_cvt_pk_bf16_f32 v72, v48, v49\n\t"
    "v_cvt_pk_bf16_f32 v73, v50, v51\n\t"
    "v_cvt_pk_bf16_f32 v74, v52, v53\n\t"
    "v_cvt_pk_bf16_f32 v75, v54, v55\n\t"
    "v_cvt_pk_bf16_f32 v76, v56, v57\n\t"
    "v_cvt_pk_bf16_f32 v77, v58, v59\n\t"
    "v_cvt_pk_bf16_f32 v78, v60, v61\n\t"
    "v_cvt_pk_bf16_f32 v79, v62, v63\n\t"
    "s_mov_b32 s6, 0x3f803f80\n\t"
    "s_mov_b32 s7, s6\n\t"
    "v_mov_b64 v[24:25], s[6:7]\n\t"
    "v_mov_b64 v[26:27], s[6:7]\n\t"
    "v_mov_b64 v[28:29], s[6:7]\n\t"
    "v_mov_b64 v[30:31], s[6:7]\n\t"
    ".LXp195_%=:\n\t"
    "s_cbranch_execz .LXp203_%=\n\t"
    "s_lshl_b32 s68, s5, 2\n\t"
    "s_add_u32 s68, s68, s58\n\t"
    "v_permlane16_swap_b32 v72, v24\n\t"
    "v_permlane16_swap_b32 v73, v25\n\t"
    "v_permlane16_swap_b32 v74, v26\n\t"
    "v_permlane16_swap_b32 v75, v27\n\t"
    "v_permlane16_swap_b32 v76, v28\n\t"
    "v_permlane16_swap_b32 v77, v29\n\t"
    "v_permlane16_swap_b32 v78, v30\n\t"
    "v_permlane16_swap_b32 v79, v31\n\t"
    "s_lshl_b32 s6, s68, 5\n\t"
    "s_add_u32 s6, s6, s4\n\t"
    "v_mfma_f32_32x32x16_bf16 v[32:47], v[64:67], v[72:75], 0\n\t"
    "v_mfma_f32_32x32x16_bf16 v[48:63], v[64:67], v[24:27], 0\n\t"
    "v_mfma_f32_32x32x16_bf16 v[32:47], v[68:71], v[76:79], v[32:47]\n\t"
    "v_mfma_f32_32x32x16_bf16 v[48:63], v[68:71], v[28:31], v[48:63]\n\t"
    "s_lshl_b32 s8, s6, 11\n\t"
    "s_add_u32 s8, s8, 5242880\n\t"
    "s_lshl_b32 s9, s6, 9\n\t"
    "s_add_u32 s9, s9, 9437184\n\t"
    "v_lshlrev_b32 v6, 3, v0\n\t"
    "s_nop 6\n\t"
    "v_pk_add_f32 v[32:33], v[32:33], v[56:57]\n\t"
    "v_pk_add_f32 v[34:35], v[34:35], v[58:59]\n\t"
    "v_pk_add_f32 v[36:37], v[36:37], v[60:61]\n\t"
    "v_pk_add_f32 v[38:39], v[38:39], v[62:63]\n\t"
    "buffer_store_dwordx4 v[32:35], v1, s[36:39], s8 offen offset:0 sc1\n\t"
    "buffer_store_dwordx4 v[36:39], v1, s[36:39], s8 offen offset:1024 sc1\n\t"
    "v_add3_u32 v40, v32, v33, v34\n\t"
    "v_add3_u32 v40, v40, v35, s56\n\t"
    "v_add3_u32 v41, v36, v37, v38\n\t"
    "v_add3_u32 v41, v41, v39, s56\n\t"
    "buffer_store_dwordx2 v[40:41], v6, s[36:39], s9 offen sc1\n\t"
    "s_waitcnt vmcnt(3)\n\t"
    "s_mov_b64 exec, 1\n\t"
    "ds_add_u32 v9, v7 offset:8\n\t"
    "s_mov_b64 exec, -1\n\t"
    "s_setprio 3\n\t"
    "s_lshl_b32 s75, s77, 7\n\t"
    "s_add_u32 s75, s75, 16384\n\t"
    "s_cmp_lg_u32 s58, 0\n\t"
    "s_cbranch_scc1 .LX125_%=\n\t"
    "s_mov_b64 exec, 1\n\t"
    "v_mov_b32 v24, s74\n\t"
    "v_add_u32 v25, v82, v24\n\t"
    "s_lshl_b32 s6, s78, 3\n\t"
    "s_add_u32 s6, s6, s75\n\t"
    "buffer_store_dwordx2 v[24:25], off, s[36:39], s6 sc1\n\t"
    "s_cmp_lg_u32 s2, 0\n\t"
    "s_cbranch_scc1 .LX126_%=\n\t"
    "buffer_store_dwordx2 v[82:83], off, s[36:39], 0 sc1\n\t"
    ".LX126_%=:\n\t"
    "s_mov_b64 exec, -1\n\t"
    ".LX125_%=:\n\t"
    "s_mov_b64 s[40:41], s[20:21]\n\t"
    "s_mov_b32 s42, 524288\n\t"
    "s_mov_b32 s43, 0x20000\n\t"
    "s_add_u32 m0, s72, 126976\n\t"
    "s_add_u32 s15, s72, 0x11000\n\t"
    "s_mov_b32 s7, 8\n\t"
    ".LX127_%=:\n\t"
    "buffer_load_dwordx4 v8, s[40:43], s71 offen lds\n\t"
    "s_mov_b32 m0, s15\n\t"
    "s_add_u32 s15, s15, 0x1000\n\t"
    "s_add_u32 s71, s71, 0x8000\n\t"
    "s_sub_u32 s7, s7, 1\n\t"
    "s_cmp_lg_u32 s7, 0\n\t"
    "s_cbranch_scc1 .LX127_%=\n\t"
    "s_lshl_b32 s6, s68, 16\n\t"
    "s_lshl_b32 s14, s4, 6\n\t"
    "s_add_u32 s8, s6, s14\n\t"
    "s_add_u32 s8, s8, 5242880\n\t"
    "s_add_u32 s9, s8, 0x8000\n\t"
    "s_lshl_b32 s12, s68, 14\n\t"
    "s_and_b32 s6, s4, 15\n\t"
    "s_lshl_b32 s6, s6, 5\n\t"
    "s_add_u32 s12, s12, s6\n\t"
    "s_lshr_b32 s7, s4, 4\n\t"
    "s_lshl_b32 s7, s7, 2\n\t"
    "s_add_u32 s12, s12, s7\n\t"
    "s_add_u32 s12, s12, 9437184\n\t"
    "s_add_u32 s13, s12, 0x2000\n\t"
    ".LXp196_%=:\n\t"
    "s_cbranch_execz .LXr204_%=\n\t"
    "s_mov_b32 s7, 524288\n\t"
    ".LX128_%=:\n\t"
    "buffer_load_dwordx4 v[32:35], v4, s[36:39], s8 offen sc1\n\t"
    "buffer_load_dword v28, v5, s[36:39], s12 offen sc1\n\t"
    "buffer_load_dwordx4 v[36:39], v4, s[36:39], s9 offen sc1\n\t"
    "buffer_load_dword v29, v5, s[36:39], s13 offen sc1\n\t"
    "s_waitcnt vmcnt(0)\n\t"
    "v_add3_u32 v25, v32, v33, v34\n\t"
    "v_add3_u32 v25, v25, v35, s56\n\t"
    "v_xor_b32 v24, v25, v28\n\t"
    "v_add3_u32 v25, v36, v37, v38\n\t"
    "v_add3_u32 v25, v25, v39, s56\n\t"
    "v_bitop3_b32 v24, v24, v25, v29 bitop3:0xf6\n\t"
    "v_cmp_ne_u32 vcc, 0, v24\n\t"
    "s_cbranch_vccz .LX129_%=\n\t"
    "s_sleep 8\n\t"
    "s_sub_u32 s7, s7, 1\n\t"
    "s_cmp_lg_u32 s7, 0\n\t"
    "s_cbranch_scc1 .LX128_%=\n\t"
    ".LX129_%=:\n\t"
    "s_mov_b64 exec, 1\n\t"
    "ds_add_u32 v10, v13 offset:128\n\t"
    "s_mov_b64 exec, -1\n\t"
    "v_pk_add_f32 v[32:33], v[32:33], v[36:37]\n\t"
    "v_pk_add_f32 v[34:35], v[34:35], v[38:39]\n\t"
    "s_lshl_b32 s9, s68, 11\n\t"
    "s_add_u32 s9, s9, s14\n\t"
    "v_permlane32_swap_b32 v32, v34\n\t"
    "v_permlane32_swap_b32 v33, v35\n\t"
    "v_pk_add_f32 v[32:33], v[32:33], v[34:35]\n\t"
    "s_nop 1\n\t"
    "v_permlane16_swap_b32 v32, v33\n\t"
    "v_add_f32 v32, v32, v33\n\t"
    "s_nop 1\n\t"
    "v_add_f32_dpp v32, v32, v32 row_ror:4 row_mask:0xf bank_mask:0xf\n\t"
    "s_nop 1\n\t"
    "v_add_f32_dpp v32, v32, v32 row_ror:8 row_mask:0xf bank_mask:0xf\n\t"
    "v_mov_b32 v33, v32\n\t"
    "s_lshl_b32 s8, s63, 12\n\t"
    "s_add_u32 s8, s8, 262144\n\t"
    "v_permlane16_swap_b32 v32, v33\n\t"
    "v_cvt_pk_bf16_f32 v42, v32, v33\n\t"
    "v_mov_b32 v43, v42\n\t"
    "s_add_u32 s9, s9, 262144\n\t"
    "s_nop 0\n\t"
    "v_permlane32_swap_b32 v42, v43\n\t"
    "v_add3_u32 v41, v42, v43, s56\n\t"
    "v_mov_b32 v44, v41\n\t"
    "s_mov_b64 exec, 15\n\t"
    "s_bitcmp1_b32 s4, 4\n\t"
    "s_cbranch_scc1 .LX130_%=\n\t"
    "buffer_store_dwordx4 v[40:43], v1, s[36:39], s9 offen sc1\n\t"
    "s_branch .LX131_%=\n\t"
    ".LX130_%=:\n\t"
    "buffer_store_dwordx4 v[42:45], v1, s[36:39], s9 offen sc1\n\t"
    ".LX131_%=:\n\t"
    "s_mov_b64 exec, -1\n\t"
    "v_xor_b32 v3, 64, v11\n\t"
    "s_cmp_lg_u32 s59, 0\n\t"
    "s_cbranch_scc1 .LX132_%=\n\t"
    "s_mov_b32 s7, 4194304\n\t"
    ".LXw135_%=:\n\t"
    "ds_read_b32 v14, v10 offset:128\n\t"
    "s_waitcnt lgkmcnt(0)\n\t"
    "v_readfirstlane_b32 s6, v14\n\t"
    "s_cmp_ge_u32 s6, 4\n\t"
    "s_cbranch_scc1 .LXd136_%=\n\t"
    "s_sleep 2\n\t"
    "s_sub_u32 s7, s7, 1\n\t"
    "s_cmp_lg_u32 s7, 0\n\t"
    "s_cbranch_scc1 .LXw135_%=\n\t"
    ".LXd136_%=:\n\t"
    "ds_read_b64_tr_b16 a[0:1], v11 offset:61440\n\t"
    "ds_read_b64_tr_b16 a[2:3], v11 offset:62464\n\t"
    "ds_read_b64_tr_b16 a[4:5], v3 offset:61440\n\t"
    "ds_read_b64_tr_b16 a[6:7], v3 offset:62464\n\t"
    "ds_read_b64_tr_b16 a[8:9], v11 offset:63488\n\t"
    "ds_read_b64_tr_b16 a[10:11], v11 offset:64512\n\t"
    "ds_read_b64_tr_b16 a[12:13], v3 offset:63488\n\t"
    "ds_read_b64_tr_b16 a[14:15], v3 offset:64512\n\t"
    "s_waitcnt lgkmcnt(0)\n\t"
    ".LX132_%=:\n\t"
    "s_mov_b64 exec, 1\n\t"
    "ds_add_u32 v10, v13 offset:144\n\t"
    ".LXp197_%=:\n\t"
    "s_cbranch_execz .LXr204_%=\n\t"
    "s_mov_b64 exec, -1\n\t"
    "s_mov_b32 s7, 4194304\n\t"
    ".LXw137_%=:\n\t"
    "ds_read_b32 v14, v10 offset:144\n\t"
    "s_waitcnt lgkmcnt(0)\n\t"
    "v_readfirstlane_b32 s6, v14\n\t"
    "s_cmp_ge_u32 s6, 4\n\t"
    "s_cbranch_scc1 .LXd138_%=\n\t"
    "s_sleep 2\n\t"
    "s_sub_u32 s7, s7, 1\n\t"
    "s_cmp_lg_u32 s7, 0\n\t"
    "s_cbranch_scc1 .LXw137_%=\n\t"
    ".LXd138_%=:\n\t"
    "s_add_u32 m0, s72, 0x18000\n\t"
    "s_add_u32 s15, s72, 0x19000\n\t"
    "s_mov_b32 s7, 8\n\t"
    ".LX139_%=:\n\t"
    "buffer_load_dwordx4 v8, s[40:43], s71 offen lds\n\t"
    "s_mov_b32 m0, s15\n\t"
    "s_add_u32 s15, s15, 0x1000\n\t"
    "s_add_u32 s71, s71, 0x8000\n\t"
    "s_sub_u32 s7, s7, 1\n\t"
    "s_cmp_lg_u32 s7, 0\n\t"
    "s_cbranch_scc1 .LX139_%=\n\t"
    "s_mov_b32 s7, 524288\n\t"
    "s_sleep 4\n\t"
    ".LX140_%=:\n\t"
    "buffer_load_dwordx4 v[64:67], v1, s[36:39], s8 offen offset:0 sc1\n\t"
    "buffer_load_dwordx4 v[68:71], v1, s[36:39], s8 offen offset:1024 sc1\n\t"
    "buffer_load_dwordx4 v[72:75], v1, s[36:39], s8 offen offset:2048 sc1\n\t"
    "buffer_load_dwordx4 v[76:79], v1, s[36:39], s8 offen offset:3072 sc1\n\t"
    "s_waitcnt vmcnt(0)\n\t"
    "v_add3_u32 v24, v66, v67, s56\n\t"
    "v_xor_b32 v24, v24, v65\n\t"
    "v_add3_u32 v25, v68, v69, s56\n\t"
    "v_bitop3_b32 v24, v24, v25, v70 bitop3:0xf6\n\t"
    "v_add3_u32 v25, v74, v75, s56\n\t"
    "v_bitop3_b32 v24, v24, v25, v73 bitop3:0xf6\n\t"
    "v_add3_u32 v25, v76, v77, s56\n\t"
    "v_bitop3_b32 v24, v24, v25, v78 bitop3:0xf6\n\t"
    "v_cmp_ne_u32 vcc, 0, v24\n\t"
    "s_cbranch_vccz .LX141_%=\n\t"
    "s_sleep 8\n\t"
    "s_sub_u32 s7, s7, 1\n\t"
    "s_cmp_lg_u32 s7, 0\n\t"
    "s_cbranch_scc1 .LX140_%=\n\t"
    ".LX141_%=:\n\t"
    "v_mfma_f32_32x32x16_bf16 v[32:47], v[66:69], v[16:19], 0\n\t"
    "v_mfma_f32_32x32x16_bf16 v[48:63], v[74:77], v[20:23], 0\n\t"
    "s_lshl_b32 s10, s63, 10\n\t"
    "s_add_u32 s10, s10, s70\n\t"
    "s_add_u32 s10, s10, 1048576\n\t"
    "s_nop 7\n\t"
    "s_nop 0\n\t"
    "v_add_f32 v40, 897988541, v40\n\t"
    "v_add_f32 v56, 897988541, v56\n\t"
    "v_rcp_f32 v40, v40\n\t"
    "v_rcp_f32 v56, v56\n\t"
    "v_pk_mul_f32 v[32:33], v[32:33], v[40:41] op_sel_hi:[1,0]\n\t"
    "v_pk_mul_f32 v[34:35], v[34:35], v[40:41] op_sel_hi:[1,0]\n\t"
    "v_pk_mul_f32 v[36:37], v[36:37], v[40:41] op_sel_hi:[1,0]\n\t"
    "v_pk_mul_f32 v[38:39], v[38:39], v[40:41] op_sel_hi:[1,0]\n\t"
    "v_pk_mul_f32 v[48:49], v[48:49], v[56:57] op_sel_hi:[1,0]\n\t"
    "v_pk_mul_f32 v[50:51], v[50:51], v[56:57] op_sel_hi:[1,0]\n\t"
    "v_pk_mul_f32 v[52:53], v[52:53], v[56:57] op_sel_hi:[1,0]\n\t"
    "v_pk_mul_f32 v[54:55], v[54:55], v[56:57] op_sel_hi:[1,0]\n\t"
    "v_cvt_pk_bf16_f32 v32, v32, v33\n\t"
    "v_cvt_pk_bf16_f32 v33, v34, v35\n\t"
    "v_cvt_pk_bf16_f32 v34, v36, v37\n\t"
    "v_cvt_pk_bf16_f32 v35, v38, v39\n\t"
    "v_cvt_pk_bf16_f32 v36, v48, v49\n\t"
    "v_cvt_pk_bf16_f32 v37, v50, v51\n\t"
    "v_cvt_pk_bf16_f32 v38, v52, v53\n\t"
    "v_cvt_pk_bf16_f32 v39, v54, v55\n\t"
    "buffer_store_dwordx4 v[32:35], v1, s[36:39], s10 offen sc1\n\t"
    "buffer_store_dwordx4 v[36:39], v1, s[36:39], s10 offen offset:1024 sc1\n\t"
    "v_accvgpr_read_b32 v8, a80\n\t"
    "s_lshl_b32 s6, s69, 4\n\t"
    "s_add_u32 s6, s6, s63\n\t"
    "s_lshl_b32 s6, s6, 3\n\t"
    ".LXp198_%=:\n\t"
    "s_cbranch_execz .LXr204_%=\n\t"
    "s_add_u32 s8, s6, 8192\n\t"
    "s_waitcnt vmcnt(0)\n\t"
    "s_mov_b64 exec, 1\n\t"
    "buffer_store_dwordx2 v[82:83], off, s[36:39], s8 sc1\n\t"
    "s_mov_b64 exec, -1\n\t"
    "s_mov_b32 s7, 4194304\n\t"
    ".LXw146_%=:\n\t"
    "ds_read_b32 v14, v10 offset:36\n\t"
    "s_waitcnt lgkmcnt(0)\n\t"
    "v_readfirstlane_b32 s6, v14\n\t"
    "s_cmp_ge_u32 s6, 4\n\t"
    "s_cbranch_scc1 .LXd147_%=\n\t"
    "s_sleep 8\n\t"
    "s_sub_u32 s7, s7, 1\n\t"
    "s_cmp_lg_u32 s7, 0\n\t"
    "s_cbranch_scc1 .LXw146_%=\n\t"
    ".LXd147_%=:\n\t"
    "s_mov_b32 s7, 4194304\n\t"
    ".LXw148_%=:\n\t"
    "ds_read_b32 v14, v10 offset:100\n\t"
    "s_waitcnt lgkmcnt(0)\n\t"
    "v_readfirstlane_b32 s6, v14\n\t"
    "s_cmp_ge_u32 s6, 4\n\t"
    "s_cbranch_scc1 .LXd149_%=\n\t"
    "s_sleep 8\n\t"
    "s_sub_u32 s7, s7, 1\n\t"
    "s_cmp_lg_u32 s7, 0\n\t"
    "s_cbranch_scc1 .LXw148_%=\n\t"
    ".LXd149_%=:\n\t"
    "s_mov_b64 s[40:41], s[30:31]\n\t"
    "s_mov_b32 s42, 524288\n\t"
    "s_mov_b32 s43, 0x20000\n\t"
    "s_sub_u32 s71, s71, 0x80000\n\t"
    "s_add_u32 m0, s72, 0x8000\n\t"
    "s_add_u32 s15, s72, 0x9000\n\t"
    "s_mov_b32 s7, 8\n\t"
    ".LX150_%=:\n\t"
    "buffer_load_dwordx4 v8, s[40:43], s71 offen lds\n\t"
    "s_mov_b32 m0, s15\n\t"
    "s_add_u32 s15, s15, 0x1000\n\t"
    "s_add_u32 s71, s71, 0x8000\n\t"
    "s_sub_u32 s7, s7, 1\n\t"
    "s_cmp_lg_u32 s7, 0\n\t"
    "s_cbranch_scc1 .LX150_%=\n\t"
    "v_and_b32 v6, 0x78, v6\n\t"
    "s_lshl_b32 s6, s77, 1\n\t"
    "s_add_u32 s6, s6, s62\n\t"
    "s_lshl_b32 s6, s6, 7\n\t"
    "s_add_u32 s8, s6, 8192\n\t"
    "s_cmp_lg_u32 s58, 0\n\t"
    "s_cbranch_scc1 .LX142_%=\n\t"
    "buffer_load_dwordx2 v[26:27], v6, s[36:39], s75 offen sc1\n\t"
    ".LX142_%=:\n\t"
    "s_mov_b32 s7, 524288\n\t"
    ".LX143_%=:\n\t"
    "buffer_load_dwordx2 v[24:25], v6, s[36:39], s8 offen sc1\n\t"
    "s_waitcnt vmcnt(0)\n\t"
    "v_cmp_ne_u64 vcc, v[24:25], v[82:83]\n\t"
    "s_cbranch_vccz .LX144_%=\n\t"
    "s_sleep 8\n\t"
    "s_sub_u32 s7, s7, 1\n\t"
    "s_cmp_lg_u32 s7, 0\n\t"
    "s_cbranch_scc1 .LX143_%=\n\t"
    ".LX144_%=:\n\t"
    "s_cmp_lg_u32 s58, 0\n\t"
    "s_cbranch_scc1 .LX145_%=\n\t"
    "v_add_u32 v28, v82, v26\n\t"
    "v_subrev_u32 v26, s74, v26\n\t"
    "v_cmp_eq_u32 vcc, v28, v27\n\t"
    "s_mov_b64 exec, 0xff\n\t"
    "v_med3_i32 v26, v26, 0, 60\n\t"
    "v_cndmask_b32 v26, 0, v26, vcc\n\t"
    "ds_max_u32 v10, v26 offset:156\n\t"
    ".LX145_%=:\n\t"
    "s_mov_b64 exec, 1\n\t"
    "ds_add_u32 v10, v13 offset:192\n\t"
    "s_mov_b64 exec, -1\n\t"
    "s_setprio 3\n\t"
    "s_mov_b32 s7, 4194304\n\t"
    ".LXw151_%=:\n\t"
    "ds_read_b32 v14, v10 offset:40\n\t"
    "s_waitcnt lgkmcnt(0)\n\t"
    "v_readfirstlane_b32 s6, v14\n\t"
    "s_cmp_ge_u32 s6, 4\n\t"
    "s_cbranch_scc1 .LXd152_%=\n\t"
    "s_sleep 8\n\t"
    "s_sub_u32 s7, s7, 1\n\t"
    "s_cmp_lg_u32 s7, 0\n\t"
    "s_cbranch_scc1 .LXw151_%=\n\t"
    ".LXd152_%=:\n\t"
    "s_mov_b32 s7, 4194304\n\t"
    ".LXw153_%=:\n\t"
    "ds_read_b32 v14, v10 offset:104\n\t"
    "s_waitcnt lgkmcnt(0)\n\t"
    "v_readfirstlane_b32 s6, v14\n\t"
    "s_cmp_ge_u32 s6, 4\n\t"
    "s_cbranch_scc1 .LXd154_%=\n\t"
    "s_sleep 8\n\t"
    "s_sub_u32 s7, s7, 1\n\t"
    ".LXp199_%=:\n\t"
    "s_cbranch_execz .LXr204_%=\n\t"
    "s_cmp_lg_u32 s7, 0\n\t"
    "s_cbranch_scc1 .LXw153_%=\n\t"
    ".LXd154_%=:\n\t"
    "s_mov_b32 m0, s72\n\t"
    "s_add_u32 s15, s72, 0x1000\n\t"
    "s_mov_b32 s7, 8\n\t"
    ".LX155_%=:\n\t"
    "buffer_load_dwordx4 v8, s[40:43], s71 offen lds\n\t"
    "s_mov_b32 m0, s15\n\t"
    "s_add_u32 s15, s15, 0x1000\n\t"
    "s_add_u32 s71, s71, 0x8000\n\t"
    "s_sub_u32 s7, s7, 1\n\t"
    "s_cmp_lg_u32 s7, 0\n\t"
    "s_cbranch_scc1 .LX155_%=\n\t"
    "s_lshl_b32 s6, s77, 2\n\t"
    "s_add_u32 s6, s6, s58\n\t"
    "s_lshl_b32 s6, s6, 14\n\t"
    "s_add_u32 s6, s6, 1048576\n\t"
    "buffer_load_dwordx4 v[16:19], v1, s[36:39], s6 offen offset:0 sc1\n\t"
    "buffer_load_dwordx4 v[20:23], v1, s[36:39], s6 offen offset:1024 sc1\n\t"
    "buffer_load_dwordx4 v[24:27], v1, s[36:39], s6 offen offset:2048 sc1\n\t"
    "buffer_load_dwordx4 v[28:31], v1, s[36:39], s6 offen offset:3072 sc1\n\t"
    "s_add_u32 s6, s6, 0x1000\n\t"
    "buffer_load_dwordx4 v[32:35], v1, s[36:39], s6 offen offset:0 sc1\n\t"
    "buffer_load_dwordx4 v[36:39], v1, s[36:39], s6 offen offset:1024 sc1\n\t"
    "buffer_load_dwordx4 v[40:43], v1, s[36:39], s6 offen offset:2048 sc1\n\t"
    "buffer_load_dwordx4 v[44:47], v1, s[36:39], s6 offen offset:3072 sc1\n\t"
    "s_add_u32 s6, s6, 0x1000\n\t"
    "buffer_load_dwordx4 v[48:51], v1, s[36:39], s6 offen offset:0 sc1\n\t"
    "buffer_load_dwordx4 v[52:55], v1, s[36:39], s6 offen offset:1024 sc1\n\t"
    "buffer_load_dwordx4 v[56:59], v1, s[36:39], s6 offen offset:2048 sc1\n\t"
    "buffer_load_dwordx4 v[60:63], v1, s[36:39], s6 offen offset:3072 sc1\n\t"
    "s_add_u32 s6, s6, 0x1000\n\t"
    "buffer_load_dwordx4 v[64:67], v1, s[36:39], s6 offen offset:0 sc1\n\t"
    "buffer_load_dwordx4 v[68:71], v1, s[36:39], s6 offen offset:1024 sc1\n\t"
    "buffer_load_dwordx4 v[72:75], v1, s[36:39], s6 offen offset:2048 sc1\n\t"
    "buffer_load_dwordx4 v[76:79], v1, s[36:39], s6 offen offset:3072 sc1\n\t"
    "s_add_u32 s6, s6, 0x1000\n\t"
    "s_waitcnt vmcnt(16)\n\t"
    "s_mov_b64 exec, 1\n\t"
    "ds_add_u32 v10, v13 offset:196\n\t"
    "s_mov_b64 exec, -1\n\t"
    "v_lshl_add_u32 v6, s58, 2, v10\n\t"
    "s_mov_b32 s7, 4194304\n\t"
    ".LXw156_%=:\n\t"
    "ds_read_b32 v14, v10 offset:192\n\t"
    "s_waitcnt lgkmcnt(0)\n\t"
    "v_readfirstlane_b32 s6, v14\n\t"
    "s_cmp_ge_u32 s6, 4\n\t"
    "s_cbranch_scc1 .LXd157_%=\n\t"
    "s_sleep 4\n\t"
    "s_sub_u32 s7, s7, 1\n\t"
    "s_cmp_lg_u32 s7, 0\n\t"
    "s_cbranch_scc1 .LXw156_%=\n\t"
    ".LXd157_%=:\n\t"
    "s_mov_b32 s7, 4194304\n\t"
    ".LXw158_%=:\n\t"
    "ds_read_b32 v14, v6 offset:160\n\t"
    "s_waitcnt lgkmcnt(0)\n\t"
    "v_readfirstlane_b32 s6, v14\n\t"
    "s_cmp_ge_u32 s6, 1\n\t"
    "s_cbranch_scc1 .LXd159_%=\n\t"
    "s_sleep 8\n\t"
    "s_sub_u32 s7, s7, 1\n\t"
    "s_cmp_lg_u32 s7, 0\n\t"
    "s_cbranch_scc1 .LXw158_%=\n\t"
    ".LXd159_%=:\n\t"
    "s_setprio 0\n\t"
    "s_cmp_eq_u32 s59, 0\n\t"
    "s_cbranch_scc1 .LX133_%=\n\t"
    "ds_read_b64_tr_b16 a[0:1], v11 offset:0\n\t"
    "ds_read_b64_tr_b16 a[2:3], v11 offset:1024\n\t"
    "ds_read_b64_tr_b16 a[4:5], v3 offset:0\n\t"
    "ds_read_b64_tr_b16 a[6:7], v3 offset:1024\n\t"
    "ds_read_b64_tr_b16 a[8:9], v11 offset:2048\n\t"
    "ds_read_b64_tr_b16 a[10:11], v11 offset:3072\n\t"
    "ds_read_b64_tr_b16 a[12:13], v3 offset:2048\n\t"
    "ds_read_b64_tr_b16 a[14:15], v3 offset:3072\n\t"
    ".LX133_%=:\n\t"
    "ds_read_b64_tr_b16 a[16:17], v11 offset:4096\n\t"
    "ds_read_b64_tr_b16 a[18:19], v11 offset:5120\n\t"
    "ds_read_b64_tr_b16 a[20:21], v3 offset:4096\n\t"
    "ds_read_b64_tr_b16 a[22:23], v3 offset:5120\n\t"
    "ds_read_b64_tr_b16 a[24:25], v11 offset:6144\n\t"
    ".LXp200_%=:\n\t"
    "s_cbranch_execz .LXr204_%=\n\t"
    "ds_read_b64_tr_b16 a[26:27], v11 offset:7168\n\t"
    "ds_read_b64_tr_b16 a[28:29], v3 offset:6144\n\t"
    "ds_read_b64_tr_b16 a[30:31], v3 offset:7168\n\t"
    "ds_read_b64_tr_b16 a[32:33], v11 offset:8192\n\t"
    "ds_read_b64_tr_b16 a[34:35], v11 offset:9216\n\t"
    "ds_read_b64_tr_b16 a[36:37], v3 offset:8192\n\t"
    "ds_read_b64_tr_b16 a[38:39], v3 offset:9216\n\t"
    "ds_read_b64_tr_b16 a[40:41], v11 offset:10240\n\t"
    "ds_read_b64_tr_b16 a[42:43], v11 offset:11264\n\t"
    "ds_read_b64_tr_b16 a[44:45], v3 offset:10240\n\t"
    "ds_read_b64_tr_b16 a[46:47], v3 offset:11264\n\t"
    "s_mov_b64 exec, 1\n\t"
    "ds_add_u32 v10, v13 offset:204\n\t"
    "s_mov_b64 exec, -1\n\t"
    "s_lshl_b32 s6, s59, 3\n\t"
    "s_add_u32 s6, s6, s62\n\t"
    "s_lshl_b32 s6, s6, 12\n\t"
    "s_add_u32 s6, s6, 0x11000\n\t"
    "v_add_u32 v4, s6, v1\n\t"
    "v_xor_b32 v5, 0x8000, v4\n\t"
    "v_xor_b32 v7, 4, v6\n\t"
    "v_and_b32 v2, 31, v0\n\t"
    "v_lshlrev_b32 v2, 11, v2\n\t"
    "v_lshrrev_b32 v9, 5, v0\n\t"
    "v_lshl_add_u32 v2, v9, 4, v2\n\t"
    "s_lshl_b32 s6, s77, 1\n\t"
    "s_add_u32 s6, s6, s62\n\t"
    "s_lshl_b32 s6, s6, 16\n\t"
    "s_lshl_b32 s8, s78, 1\n\t"
    "s_add_u32 s8, s8, s59\n\t"
    "s_lshl_b32 s8, s8, 6\n\t"
    "s_add_u32 s6, s6, s8\n\t"
    "v_add_u32 v2, s6, v2\n\t"
    "s_waitcnt vmcnt(0)\n\t"
    "s_mov_b32 s7, 4194304\n\t"
    ".LXw160_%=:\n\t"
    "ds_read_b32 v14, v10 offset:204\n\t"
    "s_waitcnt lgkmcnt(0)\n\t"
    "v_readfirstlane_b32 s6, v14\n\t"
    "s_cmp_ge_u32 s6, 4\n\t"
    "s_cbranch_scc1 .LXd161_%=\n\t"
    "s_sleep 4\n\t"
    "s_sub_u32 s7, s7, 1\n\t"
    "s_cmp_lg_u32 s7, 0\n\t"
    "s_cbranch_scc1 .LXw160_%=\n\t"
    ".LXd161_%=:\n\t"
    "s_mov_b32 s7, 4194304\n\t"
    ".LXw162_%=:\n\t"
    "ds_read_b32 v14, v6 offset:160\n\t"
    "s_waitcnt lgkmcnt(0)\n\t"
    "v_readfirstlane_b32 s6, v14\n\t"
    "s_cmp_ge_u32 s6, 4096\n\t"
    "s_cbranch_scc1 .LXd163_%=\n\t"
    "s_sleep 2\n\t"
    "s_sub_u32 s7, s7, 1\n\t"
    "s_cmp_lg_u32 s7, 0\n\t"
    "s_cbranch_scc1 .LXw162_%=\n\t"
    ".LXd163_%=:\n\t"
    "s_setprio 0\n\t"
    "v_mfma_f32_32x32x16_bf16 a[64:79], a[4:7], v[16:19], 0\n\t"
    "v_mfma_f32_32x32x16_bf16 a[48:63], a[0:3], v[16:19], 0\n\t"
    "v_mfma_f32_32x32x16_bf16 a[64:79], a[12:15], v[20:23], a[64:79]\n\t"
    "ds_read_b64_tr_b16 a[0:1], v11 offset:12288\n\t"
    "ds_read_b64_tr_b16 a[2:3], v11 offset:13312\n\t"
    "v_mfma_f32_32x32x16_bf16 a[48:63], a[8:11], v[20:23], a[48:63]\n\t"
    "ds_read_b64_tr_b16 a[4:5], v3 offset:12288\n\t"
    "ds_read_b64_tr_b16 a[6:7], v3 offset:13312\n\t"
    "v_mfma_f32_32x32x16_bf16 a[64:79], a[20:23], v[24:27], a[64:79]\n\t"
    "ds_read_b64_tr_b16 a[8:9], v11 offset:14336\n\t"
    "ds_read_b64_tr_b16 a[10:11], v11 offset:15360\n\t"
    "v_mfma_f32_32x32x16_bf16 a[48:63], a[16:19], v[24:27], a[48:63]\n\t"
    "ds_read_b64_tr_b16 a[12:13], v3 offset:14336\n\t"
    "ds_read_b64_tr_b16 a[14:15], v3 offset:15360\n\t"
    "v_mfma_f32_32x32x16_bf16 a[64:79], a[28:31], v[28:31], a[64:79]\n\t"
    "ds_read_b64_tr_b16 a[16:17], v11 offset:16384\n\t"
    "ds_read_b64_tr_b16 a[18:19], v11 offset:17408\n\t"
    "v_mfma_f32_32x32x16_bf16 a[48:63], a[24:27], v[28:31], a[48:63]\n\t"
    "ds_read_b64_tr_b16 a[20:21], v3 offset:16384\n\t"
    "ds_read_b64_tr_b16 a[22:23], v3 offset:17408\n\t"
    ".LXp201_%=:\n\t"
    "s_cbranch_execz .LXr204_%=\n\t"
    "v_mfma_f32_32x32x16_bf16 a[64:79], a[36:39], v[32:35], a[64:79]\n\t"
    "ds_read_b64_tr_b16 a[24:25], v11 offset:18432\n\t"
    "ds_read_b64_tr_b16 a[26:27], v11 offset:19456\n\t"
    "v_mfma_f32_32x32x16_bf16 a[48:63], a[32:35], v[32:35], a[48:63]\n\t"
    "ds_read_b64_tr_b16 a[28:29], v3 offset:18432\n\t"
    "ds_read_b64_tr_b16 a[30:31], v3 offset:19456\n\t"
    "v_mfma_f32_32x32x16_bf16 a[64:79], a[44:47], v[36:39], a[64:79]\n\t"
    "ds_read_b64_tr_b16 a[32:33], v11 offset:20480\n\t"
    "ds_read_b64_tr_b16 a[34:35], v11 offset:21504\n\t"
    "v_mfma_f32_32x32x16_bf16 a[48:63], a[40:43], v[36:39], a[48:63]\n\t"
    "ds_read_b64_tr_b16 a[36:37], v3 offset:20480\n\t"
    "ds_read_b64_tr_b16 a[38:39], v3 offset:21504\n\t"
    "s_waitcnt lgkmcnt(15)\n\t"
    "v_mfma_f32_32x32x16_bf16 a[64:79], a[4:7], v[40:43], a[64:79]\n\t"
    "ds_read_b64_tr_b16 a[40:41], v11 offset:22528\n\t"
    "ds_read_b64_tr_b16 a[42:43], v11 offset:23552\n\t"
    "v_mfma_f32_32x32x16_bf16 a[48:63], a[0:3], v[40:43], a[48:63]\n\t"
    "ds_read_b64_tr_b16 a[44:45], v3 offset:22528\n\t"
    "ds_read_b64_tr_b16 a[46:47], v3 offset:23552\n\t"
    "s_waitcnt lgkmcnt(15)\n\t"
    "v_mfma_f32_32x32x16_bf16 a[64:79], a[12:15], v[44:47], a[64:79]\n\t"
    "ds_read_b64_tr_b16 a[0:1], v11 offset:24576\n\t"
    "ds_read_b64_tr_b16 a[2:3], v11 offset:25600\n\t"
    "v_mfma_f32_32x32x16_bf16 a[48:63], a[8:11], v[44:47], a[48:63]\n\t"
    "ds_read_b64_tr_b16 a[4:5], v3 offset:24576\n\t"
    "ds_read_b64_tr_b16 a[6:7], v3 offset:25600\n\t"
    "s_waitcnt lgkmcnt(15)\n\t"
    "v_mfma_f32_32x32x16_bf16 a[64:79], a[20:23], v[48:51], a[64:79]\n\t"
    "ds_read_b64_tr_b16 a[8:9], v11 offset:26624\n\t"
    "ds_read_b64_tr_b16 a[10:11], v11 offset:27648\n\t"
    "v_mfma_f32_32x32x16_bf16 a[48:63], a[16:19], v[48:51], a[48:63]\n\t"
    "ds_read_b64_tr_b16 a[12:13], v3 offset:26624\n\t"
    "ds_read_b64_tr_b16 a[14:15], v3 offset:27648\n\t"
    "s_waitcnt lgkmcnt(15)\n\t"
    "v_mfma_f32_32x32x16_bf16 a[64:79], a[28:31], v[52:55], a[64:79]\n\t"
    "ds_read_b64_tr_b16 a[16:17], v11 offset:28672\n\t"
    "ds_read_b64_tr_b16 a[18:19], v11 offset:29696\n\t"
    "v_mfma_f32_32x32x16_bf16 a[48:63], a[24:27], v[52:55], a[48:63]\n\t"
    "ds_read_b64_tr_b16 a[20:21], v3 offset:28672\n\t"
    "ds_read_b64_tr_b16 a[22:23], v3 offset:29696\n\t"
    "s_waitcnt lgkmcnt(15)\n\t"
    "v_mfma_f32_32x32x16_bf16 a[64:79], a[36:39], v[56:59], a[64:79]\n\t"
    "ds_read_b64_tr_b16 a[24:25], v11 offset:30720\n\t"
    "ds_read_b64_tr_b16 a[26:27], v11 offset:31744\n\t"
    "v_mfma_f32_32x32x16_bf16 a[48:63], a[32:35], v[56:59], a[48:63]\n\t"
    "ds_read_b64_tr_b16 a[28:29], v3 offset:30720\n\t"
    "ds_read_b64_tr_b16 a[30:31], v3 offset:31744\n\t"
    "s_waitcnt lgkmcnt(15)\n\t"
    "v_mfma_f32_32x32x16_bf16 a[64:79], a[44:47], v[60:63], a[64:79]\n\t"
    "v_mfma_f32_32x32x16_bf16 a[48:63], a[40:43], v[60:63], a[48:63]\n\t"
    "s_waitcnt lgkmcnt(12)\n\t"
    "v_mfma_f32_32x32x16_bf16 a[64:79], a[4:7], v[64:67], a[64:79]\n\t"
    "v_mfma_f32_32x32x16_bf16 a[48:63], a[0:3], v[64:67], a[48:63]\n\t"
    "s_waitcnt lgkmcnt(8)\n\t"
    "v_mfma_f32_32x32x16_bf16 a[64:79], a[12:15], v[68:71], a[64:79]\n\t"
    "v_mfma_f32_32x32x16_bf16 a[48:63], a[8:11], v[68:71], a[48:63]\n\t"
    "s_waitcnt lgkmcnt(4)\n\t"
    "v_mfma_f32_32x32x16_bf16 a[64:79], a[20:23], v[72:75], a[64:79]\n\t"
    "v_mfma_f32_32x32x16_bf16 a[48:63], a[16:19], v[72:75], a[48:63]\n\t"
    "s_waitcnt lgkmcnt(0)\n\t"
    "v_mfma_f32_32x32x16_bf16 a[64:79], a[28:31], v[76:79], a[64:79]\n\t"
    "v_mfma_f32_32x32x16_bf16 a[48:63], a[24:27], v[76:79], a[48:63]\n\t"
    "s_setprio 3\n\t"
    "s_nop 7\n\t"
    "s_nop 2\n\t"
    "ds_write_b128 v4, a[64:67] offset:0\n\t"
    "ds_write_b128 v4, a[68:71] offset:1024\n\t"
    "ds_write_b128 v4, a[72:75] offset:2048\n\t"
    ".LXp202_%=:\n\t"
    "s_cbranch_execz .LXr204_%=\n\t"
    "ds_write_b128 v4, a[76:79] offset:3072\n\t"
    "s_mov_b64 exec, 1\n\t"
    "ds_add_u32 v6, v13 offset:208\n\t"
    "s_mov_b64 exec, -1\n\t"
    "v_accvgpr_read_b32 v16, a48\n\t"
    "v_accvgpr_read_b32 v17, a49\n\t"
    "v_accvgpr_read_b32 v18, a50\n\t"
    "v_accvgpr_read_b32 v19, a51\n\t"
    "v_accvgpr_read_b32 v20, a52\n\t"
    "v_accvgpr_read_b32 v21, a53\n\t"
    "v_accvgpr_read_b32 v22, a54\n\t"
    "v_accvgpr_read_b32 v23, a55\n\t"
    "v_accvgpr_read_b32 v24, a56\n\t"
    "v_accvgpr_read_b32 v25, a57\n\t"
    "v_accvgpr_read_b32 v26, a58\n\t"
    "v_accvgpr_read_b32 v27, a59\n\t"
    "v_accvgpr_read_b32 v28, a60\n\t"
    "v_accvgpr_read_b32 v29, a61\n\t"
    "v_accvgpr_read_b32 v30, a62\n\t"
    "v_accvgpr_read_b32 v31, a63\n\t"
    "s_mov_b32 s7, 4194304\n\t"
    ".LXw164_%=:\n\t"
    "ds_read_b32 v14, v7 offset:208\n\t"
    "s_waitcnt lgkmcnt(0)\n\t"
    "v_readfirstlane_b32 s6, v14\n\t"
    "s_cmp_ge_u32 s6, 1\n\t"
    "s_cbranch_scc1 .LXd165_%=\n\t"
    "s_sleep 0\n\t"
    "s_sub_u32 s7, s7, 1\n\t"
    "s_cmp_lg_u32 s7, 0\n\t"
    "s_cbranch_scc1 .LXw164_%=\n\t"
    ".LXd165_%=:\n\t"
    "ds_read_b128 v[32:35], v5 offset:0\n\t"
    "ds_read_b128 v[36:39], v5 offset:1024\n\t"
    "ds_read_b128 v[40:43], v5 offset:2048\n\t"
    "ds_read_b128 v[44:47], v5 offset:3072\n\t"
    "ds_read_b32 v14, v10 offset:200\n\t"
    "s_waitcnt lgkmcnt(0)\n\t"
    "v_pk_add_f32 v[16:17], v[16:17], v[32:33]\n\t"
    "v_pk_add_f32 v[18:19], v[18:19], v[34:35]\n\t"
    "v_pk_add_f32 v[20:21], v[20:21], v[36:37]\n\t"
    "v_pk_add_f32 v[22:23], v[22:23], v[38:39]\n\t"
    "v_pk_add_f32 v[24:25], v[24:25], v[40:41]\n\t"
    "v_pk_add_f32 v[26:27], v[26:27], v[42:43]\n\t"
    "v_pk_add_f32 v[28:29], v[28:29], v[44:45]\n\t"
    "v_pk_add_f32 v[30:31], v[30:31], v[46:47]\n\t"
    "v_cvt_pk_bf16_f32 v16, v16, v17\n\t"
    "v_cvt_pk_bf16_f32 v17, v18, v19\n\t"
    "v_cvt_pk_bf16_f32 v18, v20, v21\n\t"
    "v_cvt_pk_bf16_f32 v19, v22, v23\n\t"
    "v_cvt_pk_bf16_f32 v20, v24, v25\n\t"
    "v_cvt_pk_bf16_f32 v21, v26, v27\n\t"
    "v_cvt_pk_bf16_f32 v22, v28, v29\n\t"
    "v_cvt_pk_bf16_f32 v23, v30, v31\n\t"
    "v_permlane32_swap_b32 v16, v18\n\t"
    "v_permlane32_swap_b32 v17, v19\n\t"
    "v_permlane32_swap_b32 v20, v22\n\t"
    "v_permlane32_swap_b32 v21, v23\n\t"
    "v_readfirstlane_b32 s6, v14\n\t"
    "s_cmp_ge_u32 s6, 8\n\t"
    "s_cbranch_scc1 .LX134_%=\n\t"
    "s_mov_b32 s7, 4194304\n\t"
    ".LXw166_%=:\n\t"
    "ds_read_b32 v14, v10 offset:200\n\t"
    "s_waitcnt lgkmcnt(0)\n\t"
    "v_readfirstlane_b32 s6, v14\n\t"
    "s_cmp_ge_u32 s6, 8\n\t"
    "s_cbranch_scc1 .LXd167_%=\n\t"
    "s_sleep 1\n\t"
    "s_sub_u32 s7, s7, 1\n\t"
    "s_cmp_lg_u32 s7, 0\n\t"
    "s_cbranch_scc1 .LXw166_%=\n\t"
    ".LXd167_%=:\n\t"
    ".LX134_%=:\n\t"
    "global_store_dwordx4 v2, v[16:19], s[32:33] nt\n\t"
    "global_store_dwordx4 v2, v[20:23], s[32:33] offset:32 nt\n\t"
    ".LXp203_%=:\n\t"
    "s_cbranch_execz .LXr204_%=\n\t"
    "s_branch .LXend5_%=\n\t"
    ".LXfb6_%=:\n\t"
    "s_cmp_lg_u32 s63, 0\n\t"
    "s_cbranch_scc1 .LX171_%=\n\t"
    "s_mov_b32 s63, 1\n\t"
    "v_mov_b32 v2, 0xff800000\n\t"
    "v_mov_b32 v3, 0xff800000\n\t"
    "s_mov_b32 s14, 0\n\t"
    ".LX168_%=:\n\t"
    "s_lshr_b32 s6, s14, 1\n\t"
    "s_lshl_b32 s6, s6, 12\n\t"
    "s_and_b32 s7, s14, 1\n\t"
    "s_lshl_b32 s7, s7, 10\n\t"
    "s_add_u32 s6, s6, s7\n\t"
    "s_add_u32 s6, s6, 65536\n\t"
    "buffer_load_dwordx4 a[40:43], v1, s[36:39], s6 offen sc1\n\t"
    "s_waitcnt vmcnt(0)\n\t"
    "v_mfma_f32_32x32x16_bf16 v[16:31], a[40:43], a[32:35], 0\n\t"
    "v_mfma_f32_32x32x16_bf16 v[32:47], a[40:43], a[36:39], 0\n\t"
    "s_nop 7\n\t"
    "s_nop 7\n\t"
    "v_max3_f32 v2, v2, v16, v17\n\t"
    "v_max3_f32 v3, v3, v32, v33\n\t"
    "v_max3_f32 v2, v2, v18, v19\n\t"
    "v_max3_f32 v3, v3, v34, v35\n\t"
    "v_max3_f32 v2, v2, v20, v21\n\t"
    "v_max3_f32 v3, v3, v36, v37\n\t"
    "v_max3_f32 v2, v2, v22, v23\n\t"
    "v_max3_f32 v3, v3, v38, v39\n\t"
    "v_max3_f32 v2, v2, v24, v25\n\t"
    "v_max3_f32 v3, v3, v40, v41\n\t"
    "v_max3_f32 v2, v2, v26, v27\n\t"
    "v_max3_f32 v3, v3, v42, v43\n\t"
    "v_max3_f32 v2, v2, v28, v29\n\t"
    "v_max3_f32 v3, v3, v44, v45\n\t"
    "v_max3_f32 v2, v2, v30, v31\n\t"
    "v_max3_f32 v3, v3, v46, v47\n\t"
    "s_add_u32 s14, s14, 1\n\t"
    "s_cmp_lt_u32 s14, 64\n\t"
    "s_cbranch_scc1 .LX168_%=\n\t"
    "v_mov_b32 v4, v2\n\t"
    "v_mov_b32 v14, v3\n\t"
    "s_nop 1\n\t"
    "v_permlane32_swap_b32 v2, v4\n\t"
    "v_permlane32_swap_b32 v3, v14\n\t"
    "v_min_f32 v48, -v2, -v4\n\t"
    "v_min_f32 v64, -v3, -v14\n\t"
    "v_mov_b32 v49, v48\n\t"
    "v_mov_b32 v65, v64\n\t"
    "v_mov_b32 v50, v48\n\t"
    "v_mov_b32 v66, v64\n\t"
    "v_mov_b32 v51, v48\n\t"
    "v_mov_b32 v67, v64\n\t"
    "v_mov_b32 v52, v48\n\t"
    "v_mov_b32 v68, v64\n\t"
    "v_mov_b32 v53, v48\n\t"
    "v_mov_b32 v69, v64\n\t"
    "v_mov_b32 v54, v48\n\t"
    "v_mov_b32 v70, v64\n\t"
    "v_mov_b32 v55, v48\n\t"
    "v_mov_b32 v71, v64\n\t"
    "v_mov_b32 v56, v48\n\t"
    "v_mov_b32 v72, v64\n\t"
    "v_mov_b32 v57, v48\n\t"
    "v_mov_b32 v73, v64\n\t"
    "v_mov_b32 v58, v48\n\t"
    "v_mov_b32 v74, v64\n\t"
    "v_mov_b32 v59, v48\n\t"
    "v_mov_b32 v75, v64\n\t"
    "v_mov_b32 v60, v48\n\t"
    "v_mov_b32 v76, v64\n\t"
    "v_mov_b32 v61, v48\n\t"
    "v_mov_b32 v77, v64\n\t"
    "v_mov_b32 v62, v48\n\t"
    "v_mov_b32 v78, v64\n\t"
    "v_mov_b32 v63, v48\n\t"
    "v_mov_b32 v79, v64\n\t"
    "v_accvgpr_write_b32 a0, 0\n\t"
    "v_accvgpr_write_b32 a1, 0\n\t"
    "v_accvgpr_write_b32 a2, 0\n\t"
    "v_accvgpr_write_b32 a3, 0\n\t"
    "v_accvgpr_write_b32 a4, 0\n\t"
    "v_accvgpr_write_b32 a5, 0\n\t"
    "v_accvgpr_write_b32 a6, 0\n\t"
    "v_accvgpr_write_b32 a7, 0\n\t"
    "v_accvgpr_write_b32 a8, 0\n\t"
    "v_accvgpr_write_b32 a9, 0\n\t"
    "v_accvgpr_write_b32 a10, 0\n\t"
    "v_accvgpr_write_b32 a11, 0\n\t"
    "v_accvgpr_write_b32 a12, 0\n\t"
    "v_accvgpr_write_b32 a13, 0\n\t"
    "v_accvgpr_write_b32 a14, 0\n\t"
    "v_accvgpr_write_b32 a15, 0\n\t"
    "v_accvgpr_write_b32 a16, 0\n\t"
    "v_accvgpr_write_b32 a17, 0\n\t"
    "v_accvgpr_write_b32 a18, 0\n\t"
    "v_accvgpr_write_b32 a19, 0\n\t"
    "v_accvgpr_write_b32 a20, 0\n\t"
    "v_accvgpr_write_b32 a21, 0\n\t"
    "v_accvgpr_write_b32 a22, 0\n\t"
    "v_accvgpr_write_b32 a23, 0\n\t"
    "v_accvgpr_write_b32 a24, 0\n\t"
    "v_accvgpr_write_b32 a25, 0\n\t"
    "v_accvgpr_write_b32 a26, 0\n\t"
    "v_accvgpr_write_b32 a27, 0\n\t"
    "v_accvgpr_write_b32 a28, 0\n\t"
    "v_accvgpr_write_b32 a29, 0\n\t"
    "v_accvgpr_write_b32 a30, 0\n\t"
    "v_accvgpr_write_b32 a31, 0\n\t"
    "v_and_b32 v2, 0xf0, v1\n\t"
    "v_lshrrev_b32 v14, 5, v0\n\t"
    "v_lshl_add_u32 v2, v14, 8, v2\n\t"
    "s_mov_b32 s14, 0\n\t"
    ".LX169_%=:\n\t"
    "s_lshr_b32 s6, s14, 1\n\t"
    "s_lshl_b32 s6, s6, 12\n\t"
    "s_and_b32 s7, s14, 1\n\t"
    "s_lshl_b32 s7, s7, 10\n\t"
    "s_add_u32 s6, s6, s7\n\t"
    "s_add_u32 s6, s6, 65536\n\t"
    "buffer_load_dwordx4 a[40:43], v1, s[36:39], s6 offen sc1\n\t"
    "s_mov_b64 exec, s[60:61]\n\t"
    "buffer_load_dwordx4 a[48:51], v2, s[36:39], s6 offen offset:2048 sc1\n\t"
    "buffer_load_dwordx4 a[52:55], v2, s[36:39], s6 offen offset:2560 sc1\n\t"
    "s_mov_b64 exec, -1\n\t"
    "s_waitcnt vmcnt(0)\n\t"
    "s_nop 4\n\t"
    "v_mfma_f32_32x32x16_bf16 v[16:31], a[40:43], a[32:35], v[48:63]\n\t"
    "v_mfma_f32_32x32x16_bf16 v[32:47], a[40:43], a[36:39], v[64:79]\n\t"
    "s_nop 7\n\t"
    "s_nop 7\n\t"
    "v_exp_f32 v16, v16\n\t"
    "v_exp_f32 v17, v17\n\t"
    "v_exp_f32 v18, v18\n\t"
    "v_exp_f32 v19, v19\n\t"
    "v_exp_f32 v20, v20\n\t"
    "v_exp_f32 v21, v21\n\t"
    "v_exp_f32 v22, v22\n\t"
    "v_exp_f32 v23, v23\n\t"
    "v_exp_f32 v24, v24\n\t"
    "v_exp_f32 v25, v25\n\t"
    "v_exp_f32 v26, v26\n\t"
    "v_exp_f32 v27, v27\n\t"
    "v_exp_f32 v28, v28\n\t"
    "v_exp_f32 v29, v29\n\t"
    "v_exp_f32 v30, v30\n\t"
    "v_exp_f32 v31, v31\n\t"
    "v_exp_f32 v32, v32\n\t"
    "v_exp_f32 v33, v33\n\t"
    "v_exp_f32 v34, v34\n\t"
    "v_exp_f32 v35, v35\n\t"
    "v_exp_f32 v36, v36\n\t"
    "v_exp_f32 v37, v37\n\t"
    "v_exp_f32 v38, v38\n\t"
    "v_exp_f32 v39, v39\n\t"
    "v_exp_f32 v40, v40\n\t"
    "v_exp_f32 v41, v41\n\t"
    "v_exp_f32 v42, v42\n\t"
    "v_exp_f32 v43, v43\n\t"
    "v_exp_f32 v44, v44\n\t"
    "v_exp_f32 v45, v45\n\t"
    "v_exp_f32 v46, v46\n\t"
    "v_exp_f32 v47, v47\n\t"
    "s_nop 1\n\t"
    "v_smfmac_f32_32x32x32_bf16 a[0:15], a[48:51], v[16:23], v12\n\t"
    "v_smfmac_f32_32x32x32_bf16 a[0:15], a[52:55], v[24:31], v12\n\t"
    "v_smfmac_f32_32x32x32_bf16 a[16:31], a[48:51], v[32:39], v12\n\t"
    "v_smfmac_f32_32x32x32_bf16 a[16:31], a[52:55], v[40:47], v12\n\t"
    "s_add_u32 s14, s14, 1\n\t"
    "s_cmp_lt_u32 s14, 64\n\t"
    "s_cbranch_scc1 .LX169_%=\n\t"
    "s_nop 7\n\t"
    "s_nop 7\n\t"
    "v_accvgpr_read_b32 v16, a0\n\t"
    "v_accvgpr_read_b32 v17, a1\n\t"
    "v_accvgpr_read_b32 v18, a2\n\t"
    "v_accvgpr_read_b32 v19, a3\n\t"
    "v_accvgpr_read_b32 v20, a4\n\t"
    "v_accvgpr_read_b32 v21, a5\n\t"
    "v_accvgpr_read_b32 v22, a6\n\t"
    "v_accvgpr_read_b32 v23, a7\n\t"
    "v_accvgpr_read_b32 v24, a8\n\t"
    "v_accvgpr_read_b32 v32, a16\n\t"
    "v_accvgpr_read_b32 v33, a17\n\t"
    "v_accvgpr_read_b32 v34, a18\n\t"
    "v_accvgpr_read_b32 v35, a19\n\t"
    "v_accvgpr_read_b32 v36, a20\n\t"
    "v_accvgpr_read_b32 v37, a21\n\t"
    "v_accvgpr_read_b32 v38, a22\n\t"
    "v_accvgpr_read_b32 v39, a23\n\t"
    "v_accvgpr_read_b32 v40, a24\n\t"
    "s_branch .LXfbr7_%=\n\t"
    ".LX171_%=:\n\t"
    "v_min_u32 v16, -2, v16\n\t"
    "v_min_u32 v17, -2, v17\n\t"
    "v_min_u32 v18, -2, v18\n\t"
    "v_min_u32 v19, -2, v19\n\t"
    "v_min_u32 v20, -2, v20\n\t"
    "v_min_u32 v21, -2, v21\n\t"
    "v_min_u32 v22, -2, v22\n\t"
    "v_min_u32 v23, -2, v23\n\t"
    "s_branch .LX40_%=\n\t"
    ".LXend5_%=:\n\t"
    : "+{v0}"(tid)
    : "{s[0:1]}"(__builtin_amdgcn_kernarg_segment_ptr()), "{s2}"(blockIdx.x)
    : "v1", "v2", "v3", "v4", "v5", "v6", "v7", "v8", "v9", "v10", "v11", "v12", "v13", "v14", "v15", "v16", "v17", "v18", "v19", "v20", "v21", "v22", "v23", "v24", "v25", "v26", "v27", "v28", "v29", "v30", "v31", "v32", "v33", "v34", "v35", "v36", "v37", "v38", "v39", "v40", "v41", "v42", "v43", "v44", "v45", "v46", "v47", "v48", "v49", "v50", "v51", "v52", "v53", "v54", "v55", "v56", "v57", "v58", "v59", "v60", "v61", "v62", "v63", "v64", "v65", "v66", "v67", "v68", "v69", "v70", "v71", "v72", "v73", "v74", "v75", "v76", "v77", "v78", "v79", "v80", "v81", "v82", "v83", "a0", "a1", "a2", "a3", "a4", "a5", "a6", "a7", "a8", "a9", "a10", "a11", "a12", "a13", "a14", "a15", "a16", "a17", "a18", "a19", "a20", "a21", "a22", "a23", "a24", "a25", "a26", "a27", "a28", "a29", "a30", "a31", "a32", "a33", "a34", "a35", "a36", "a37", "a38", "a39", "a40", "a41", "a42", "a43", "a44", "a45", "a46", "a47", "a48", "a49", "a50", "a51", "a52", "a53", "a54", "a55", "a56", "a57", "a58", "a59", "a60", "a61", "a62", "a63", "a64", "a65", "a66", "a67", "a68", "a69", "a70", "a71", "a72", "a73", "a74", "a75", "a76", "a77", "a78", "a79", "a80", "s3", "s4", "s5", "s6", "s7", "s8", "s9", "s10", "s11", "s12", "s13", "s14", "s15", "s16", "s17", "s18", "s19", "s20", "s21", "s22", "s23", "s24", "s25", "s26", "s27", "s28", "s29", "s30", "s31", "s32", "s33", "s34", "s35", "s36", "s37", "s38", "s39", "s40", "s41", "s42", "s43", "s44", "s45", "s46", "s47", "s48", "s49", "s50", "s51", "s52", "s53", "s54", "s55", "s56", "s57", "s58", "s59", "s60", "s61", "s62", "s63", "s64", "s65", "s66", "s67", "s68", "s69", "s70", "s71", "s72", "s73", "s74", "s75", "s76", "s77", "s78", "s79", "s80", "vcc", "scc", "m0", "memory");
}

extern "C" void kernel_launch(void* const* d_in, const int* in_sizes, int n_in, void* d_out, int out_size, void* d_ws, size_t ws_size,
                              hipStream_t stream) {
  (void)hipFuncSetAttribute((const void*)fused, hipFuncAttributeMaxDynamicSharedMemorySize, 163840);
  fused<<<256, 768, 163840, stream>>>(d_in[0], d_in[1], d_in[2], d_in[3], d_in[4], d_in[5], d_in[6], d_in[7], d_out, d_ws);
}
```
